```python
import torch

def _softmax(x, axis=-1):
    return torch.softmax(x, dim=axis)
def _gelu(x):
    return torch.nn.functional.gelu(x, approximate="tanh")
def _silu(x):
    return torch.nn.functional.silu(x)
def _sigmoid(x):
    return torch.sigmoid(x)
def _layernorm(x, axis=-1, eps=1e-5):
    m = x.mean(dim=axis, keepdim=True)
    v = x.var(dim=axis, keepdim=True, unbiased=False)
    return (x - m) / torch.sqrt(v + eps)
def _scatter_add(tgt, idx, src, axis=0):
    return tgt.clone().index_add_(axis, idx, src)
def _segment_sum(src, seg, n):
    out = torch.zeros((n,) + src.shape[1:], dtype=src.dtype)
    return out.index_add_(0, seg, src)
def _pad(x, pw):
    flat = tuple(v for pair in reversed(pw) for v in pair)
    return torch.nn.functional.pad(x, flat)

kernel_name = 'standalone__paged_block_table_kv_pagedattention__prompt'

def setup_inputs():

    _w = (lambda a: (lambda t: t.to(torch.bfloat16) if t.is_floating_point() else t)(torch.as_tensor(a)))
    B, Hq, Hkv, Sq, Skv, D = 1, 16, 8, 2048, 2048, 256
    bs = 16
    nbps = max(1, Skv // bs)
    num_phys = B * nbps + 4
    bt = torch.randperm(num_phys)[: B * nbps].reshape(B, nbps).to(torch.int64)
    cl = (nbps * bs - (torch.arange(1, B + 1, dtype=torch.int64) * 2654435761) % max(1, nbps * bs // 2)).to(torch.int64)
    return {
        "q": _w((lambda *s: torch.randn(*s, dtype=torch.bfloat16) * 0.8)(B, Hq, Sq, D)),
        "k_cache": _w((lambda *s: torch.randn(*s, dtype=torch.bfloat16) * 0.8)(num_phys, bs, Hkv, D)),
        "v_cache": _w((lambda *s: torch.randn(*s, dtype=torch.bfloat16) * 0.05)(num_phys, bs, Hkv, D)),
        "block_table": _w(bt),
        "context_lens": _w(cl),
    }

def reference(q, k_cache, v_cache, block_table, context_lens):
    B, Hq, Sq, D = q.shape
    bs = k_cache.shape[1]
    Hkv = k_cache.shape[2]
    nbps = block_table.shape[1]
    Skv = nbps * bs
    bt = block_table.reshape(-1)
    k = (lambda x, *d: x.permute(*d))(k_cache[bt].reshape(B, Skv, Hkv, D), 0, 2, 1, 3)
    v = (lambda x, *d: x.permute(*d))(v_cache[bt].reshape(B, Skv, Hkv, D), 0, 2, 1, 3)
    g = Hq // Hkv
    qg = q.reshape(B, Hkv, g, Sq, D)
    scores = torch.einsum("bhgqd,bhkd->bhgqk", qg, k) * (D ** -0.5)
    pos = torch.arange(Skv)
    msk = pos[None, :] < context_lens[:, None]
    scores = torch.where(msk[:, None, None, None, :], scores, -1e9)
    p = _softmax(scores, axis=-1)
    out = torch.einsum("bhgqk,bhkd->bhgqd", p, v)
    return out.reshape(B, Hq, Sq, D)
```

```cpp
#include <hip/hip_runtime.h>
#include <hip/hip_bf16.h>

constexpr int B_ = 1, HQ = 16, HKV = 8, SQ = 2048, SKV = 2048, D = 256, BS = 16, NBPS = SKV / BS;

__device__ __forceinline__ float bf2f(unsigned short x) { return __uint_as_float(((unsigned)x) << 16); }

__global__ __launch_bounds__(256) void naive_kernel(const unsigned short* __restrict__ q, const unsigned short* __restrict__ kc,
                             const unsigned short* __restrict__ vc, const int* __restrict__ bt, const int* __restrict__ cl,
                             __hip_bfloat16* __restrict__ out) {
    __shared__ float sc[4][SKV];
    const int wid = threadIdx.x >> 6, lane = threadIdx.x & 63;
    const int row = blockIdx.x * 4 + wid;
    const int h = row / SQ, kvh = h / (HQ / HKV);
    const int ctx = cl[0];
    float qf[4];
    for (int j = 0; j < 4; ++j) qf[j] = bf2f(q[(size_t)row * D + lane * 4 + j]);
    float m = -3.0e38f;
    for (int k = 0; k < SKV; ++k) {
        const int pg = bt[k / BS];
        const unsigned short* kr = kc + (((size_t)pg * BS + (k % BS)) * HKV + kvh) * D + lane * 4;
        float s = 0.f;
        for (int j = 0; j < 4; ++j) s += qf[j] * bf2f(kr[j]);
        for (int o = 32; o > 0; o >>= 1) s += __shfl_xor(s, o);
        s *= 0.0625f;
        if (!(k < ctx)) s = -1e9f;
        if (lane == 0) sc[wid][k] = s;
        m = fmaxf(m, s);
    }
    __syncthreads();
    float acc[4] = {0, 0, 0, 0}, l = 0.f;
    for (int k = 0; k < SKV; ++k) {
        const float p = __expf(sc[wid][k] - m);
        l += p;
        const int pg = bt[k / BS];
        const unsigned short* vr = vc + (((size_t)pg * BS + (k % BS)) * HKV + kvh) * D + lane * 4;
        for (int j = 0; j < 4; ++j) acc[j] += p * bf2f(vr[j]);
    }
    for (int j = 0; j < 4; ++j) out[(size_t)row * D + lane * 4 + j] = __float2bfloat16(acc[j] / l);
}

extern "C" void kernel_launch(void* const* d_in, const int* in_sizes, int n_in,
                              void* d_out, int out_size, void* d_ws, size_t ws_size,
                              hipStream_t stream) {
    naive_kernel<<<HQ * SQ / 4, 256, 0, stream>>>((const unsigned short*)d_in[0], (const unsigned short*)d_in[1],
                                                  (const unsigned short*)d_in[2], (const int*)d_in[3], (const int*)d_in[4],
                                                  (__hip_bfloat16*)d_out);
}
```

```cpp
#include <hip/hip_runtime.h>
#include <hip/hip_bf16.h>

namespace {
constexpr int HQ = 16, HKV = 8, SQ = 2048, SKV = 2048, DH = 256, BS = 16;

__device__ __forceinline__ float bf2f(unsigned short x) { return __uint_as_float(((unsigned)x) << 16); }

__device__ __forceinline__ void slow_rows(const unsigned short* __restrict__ q, const unsigned short* __restrict__ kc, const unsigned short* __restrict__ vc,
                                       const int* __restrict__ bt, int ctx, unsigned short* __restrict__ out, int h, int row0, int nrows, int d0, int lane) {
    const int kvh = h / (HQ / HKV);
    for (int r = 0; r < nrows; ++r) {
        const unsigned short* qr = q + ((size_t)h * SQ + row0 + r) * DH;
        float m = -3.0e38f, l = 0.f, acc0 = 0.f, acc1 = 0.f;
        for (int k0 = 0; k0 < SKV; k0 += 64) {
            const int key = k0 + lane;
            const unsigned short* kr = kc + (((size_t)bt[key / BS] * BS + (key % BS)) * HKV + kvh) * DH;
            float s = 0.f;
            for (int d = 0; d < DH; ++d) s = fmaf(bf2f(qr[d]), bf2f(kr[d]), s);
            s *= 0.0625f;
            if (!(key < ctx)) s = -1e9f;
            float tm = s;
            for (int o = 32; o > 0; o >>= 1) tm = fmaxf(tm, __shfl_xor(tm, o));
            const float mn = fmaxf(m, tm);
            const float alpha = __expf(m - mn);
            const float p = __expf(s - mn);
            float ps = p;
            for (int o = 32; o > 0; o >>= 1) ps += __shfl_xor(ps, o);
            l = l * alpha + ps; acc0 *= alpha; acc1 *= alpha; m = mn;
            for (int j = 0; j < 64; ++j) {
                const float pj = __shfl(p, j);
                const int kj = k0 + j;
                const unsigned short* vr = vc + (((size_t)bt[kj / BS] * BS + (kj % BS)) * HKV + kvh) * DH + d0 + 2 * lane;
                acc0 = fmaf(pj, bf2f(vr[0]), acc0); acc1 = fmaf(pj, bf2f(vr[1]), acc1);
            }
        }
        __hip_bfloat16 o0 = __float2bfloat16(acc0 / l), o1 = __float2bfloat16(acc1 / l);
        unsigned short* orow = out + ((size_t)h * SQ + row0 + r) * DH + d0 + 2 * lane;
        orow[0] = *reinterpret_cast<unsigned short*>(&o0); orow[1] = *reinterpret_cast<unsigned short*>(&o1);
    }
}
}

__global__ __launch_bounds__(256, 1) void paged_attn_prompt_kernel(const unsigned short* __restrict__ q, const unsigned short* __restrict__ kc,
                                                                   const unsigned short* __restrict__ vc, const int* __restrict__ bt,
                                                                   const int* __restrict__ cl, unsigned short* __restrict__ out,
                                                                   void* __restrict__ ws, unsigned kvbytes) {
    __shared__ __attribute__((aligned(4096))) char lds[163840];
    const unsigned wid = __builtin_amdgcn_readfirstlane(threadIdx.x >> 6);
    const unsigned blk = blockIdx.x;
    const unsigned ldsb = (unsigned)(size_t)lds;
    unsigned flag;
    asm volatile(
        "v_mbcnt_lo_u32_b32 v238, -1, 0\n\t"
        "v_mbcnt_hi_u32_b32 v238, -1, v238\n\t"
        "s_load_dword s59, %[cl], 0x0\n\t"
        "s_mov_b32 s70, %[wid]\n\t"
        "s_and_b32 s68, s70, 1\n\t"
        "s_lshr_b32 s69, s70, 1\n\t"
        "s_cmp_lg_u32 s70, 0\n\t"
        "s_cbranch_scc1 NOPF_%=\n\t"
        "s_getpc_b64 s[88:89]\n\t"
        "v_lshlrev_b32 v160, 7, v238\n\t"
        "global_load_dword v249, v160, s[88:89]\n\t"
        "v_add_u32 v160, 0x2000, v160\n\t"
        "global_load_dword v249, v160, s[88:89]\n\t"
        "v_add_u32 v160, 0x2000, v160\n\t"
        "global_load_dword v249, v160, s[88:89]\n\t"
        "v_add_u32 v160, 0x2000, v160\n\t"
        "NOPF_%=:\n\t"
        "s_and_b32 s88, %[blk], 7\n\t"
        "s_lshr_b32 s89, %[blk], 3\n\t"
        "s_lshr_b32 s90, s89, 4\n\t"
        "s_lshl_b32 s91, s88, 1\n\t"
        "s_add_u32 s90, s90, s91\n\t"
        "s_and_b32 s89, s89, 15\n\t"
        "s_lshl_b32 s90, s90, 20\n\t"
        "s_lshl_b32 s89, s89, 16\n\t"
        "s_add_u32 s92, s90, s89\n\t"
        "s_mov_b64 s[52:53], %[bt]\n\t"
        "s_and_b32 s53, s53, 0xffff\n\t"
        "s_mov_b32 s54, 512\n\t"
        "s_mov_b32 s55, 0x20000\n\t"
        "v_lshlrev_b32 v160, 4, v238\n\t"
        "s_lshl_b32 s71, s70, 2\n\t"
        "v_add_u32 v160, s71, v160\n\t"
        "buffer_load_dword v237, v160, s[52:55], 0 offen\n\t"
        "s_and_b32 s91, %[blk], 7\n\t"
        "s_lshl_b32 s91, s91, 9\n\t"
        "s_mov_b64 s[40:41], %[k]\n\t"
        "s_add_u32 s40, s40, s91\n\t"
        "s_addc_u32 s41, s41, 0\n\t"
        "s_and_b32 s41, s41, 0xffff\n\t"
        "s_sub_u32 s42, %[kvb], s91\n\t"
        "s_mov_b32 s43, 0x20000\n\t"
        "s_mov_b64 s[44:45], %[v]\n\t"
        "s_add_u32 s44, s44, s91\n\t"
        "s_addc_u32 s45, s45, 0\n\t"
        "s_and_b32 s45, s45, 0xffff\n\t"
        "s_sub_u32 s46, %[kvb], s91\n\t"
        "s_mov_b32 s47, 0x20000\n\t"
        "s_mov_b64 s[76:77], %[o]\n\t"
        "s_and_b32 s77, s77, 0xffff\n\t"
        "s_mov_b32 s78, 0x1000000\n\t"
        "s_mov_b32 s79, 0x20000\n\t"
        "s_cmp_eq_u32 s70, 1\n\t"
        "s_cselect_b64 s[76:77], s[40:41], s[76:77]\n\t"
        "s_cselect_b64 s[78:79], s[42:43], s[78:79]\n\t"
        "s_cmp_eq_u32 s70, 2\n\t"
        "s_cselect_b64 s[76:77], s[44:45], s[76:77]\n\t"
        "s_cselect_b64 s[78:79], s[46:47], s[78:79]\n\t"
        "v_lshlrev_b32 v160, 21, v238\n\t"
        "buffer_load_dword v249, v160, s[76:79], 0 offen\n\t"
        "s_mov_b64 s[48:49], %[q]\n\t"
        "s_add_u32 s48, s48, s92\n\t"
        "s_addc_u32 s49, s49, 0\n\t"
        "s_and_b32 s49, s49, 0xffff\n\t"
        "s_mov_b32 s50, 0x10000\n\t"
        "s_mov_b32 s51, 0x20000\n\t"
        "v_bfe_u32 v160, v238, 2, 3\n\t"
        "v_lshrrev_b32 v161, 5, v238\n\t"
        "v_and_b32 v162, 3, v238\n\t"
        "v_bfe_u32 v163, v238, 4, 1\n\t"
        "v_lshlrev_b32 v164, 1, v163\n\t"
        "v_xor_b32 v165, 3, v164\n\t"
        "v_xor_b32 v164, v164, v162\n\t"
        "v_xor_b32 v165, v165, v162\n\t"
        "v_lshl_add_u32 v164, v161, 2, v164\n\t"
        "v_lshl_add_u32 v165, v161, 2, v165\n\t"
        "v_lshlrev_b32 v164, 4, v164\n\t"
        "v_lshlrev_b32 v165, 4, v165\n\t"
        "v_lshl_add_u32 v166, v160, 9, v164\n\t"
        "v_lshl_add_u32 v167, v160, 9, v165\n\t"
        "v_add_u32 v167, 0x1000, v167\n\t"
        "v_lshl_add_u32 v235, v160, 12, v164\n\t"
        "v_lshl_add_u32 v236, v160, 12, v165\n\t"
        "v_add_u32 v236, 0x8000, v236\n\t"
        "s_lshl_b32 s62, s70, 13\n\t"
        "s_add_u32 s62, s62, %[lds]\n\t"
        "s_lshl_b32 s86, s70, 14\n\t"
        "s_add_u32 s85, s86, %[lds]\n\t"
        "s_add_u32 s87, s86, 0x2000\n\t"
        "s_add_u32 m0, s85, 0x18000\n\t"
        "v_accvgpr_write_b32 a0, 0\n\t"
        "buffer_load_dwordx4 v166, s[48:51], s86 offen offset:0 nt lds\n\t"
        "v_accvgpr_write_b32 a1, 0\n\t"
        "v_accvgpr_write_b32 a2, 0\n\t"
        "v_accvgpr_write_b32 a3, 0\n\t"
        "s_add_u32 m0, s85, 0x19000\n\t"
        "v_accvgpr_write_b32 a4, 0\n\t"
        "buffer_load_dwordx4 v167, s[48:51], s86 offen offset:0 nt lds\n\t"
        "v_accvgpr_write_b32 a5, 0\n\t"
        "v_accvgpr_write_b32 a6, 0\n\t"
        "v_accvgpr_write_b32 a7, 0\n\t"
        "s_add_u32 m0, s85, 0x1a000\n\t"
        "v_accvgpr_write_b32 a8, 0\n\t"
        "buffer_load_dwordx4 v166, s[48:51], s87 offen offset:0 nt lds\n\t"
        "v_accvgpr_write_b32 a9, 0\n\t"
        "v_accvgpr_write_b32 a10, 0\n\t"
        "v_accvgpr_write_b32 a11, 0\n\t"
        "s_add_u32 m0, s85, 0x1b000\n\t"
        "v_accvgpr_write_b32 a12, 0\n\t"
        "buffer_load_dwordx4 v167, s[48:51], s87 offen offset:0 nt lds\n\t"
        "v_accvgpr_write_b32 a13, 0\n\t"
        "v_accvgpr_write_b32 a14, 0\n\t"
        "v_accvgpr_write_b32 a15, 0\n\t"
        "s_add_u32 m0, s85, 0x18380\n\t"
        "v_accvgpr_write_b32 a16, 0\n\t"
        "buffer_load_dwordx4 v166, s[48:51], s86 offen offset:128 nt lds\n\t"
        "v_accvgpr_write_b32 a17, 0\n\t"
        "v_accvgpr_write_b32 a18, 0\n\t"
        "v_accvgpr_write_b32 a19, 0\n\t"
        "s_add_u32 m0, s85, 0x19380\n\t"
        "v_accvgpr_write_b32 a20, 0\n\t"
        "buffer_load_dwordx4 v167, s[48:51], s86 offen offset:128 nt lds\n\t"
        "v_accvgpr_write_b32 a21, 0\n\t"
        "v_accvgpr_write_b32 a22, 0\n\t"
        "v_accvgpr_write_b32 a23, 0\n\t"
        "s_add_u32 m0, s85, 0x1a380\n\t"
        "v_accvgpr_write_b32 a24, 0\n\t"
        "buffer_load_dwordx4 v166, s[48:51], s87 offen offset:128 nt lds\n\t"
        "v_accvgpr_write_b32 a25, 0\n\t"
        "v_accvgpr_write_b32 a26, 0\n\t"
        "v_accvgpr_write_b32 a27, 0\n\t"
        "s_add_u32 m0, s85, 0x1b380\n\t"
        "v_accvgpr_write_b32 a28, 0\n\t"
        "buffer_load_dwordx4 v167, s[48:51], s87 offen offset:128 nt lds\n\t"
        "v_accvgpr_write_b32 a29, 0\n\t"
        "v_accvgpr_write_b32 a30, 0\n\t"
        "v_accvgpr_write_b32 a31, 0\n\t"
        "s_and_b32 s91, %[blk], 7\n\t"
        "s_lshl_b32 s91, s91, 9\n\t"
        "s_mov_b64 s[40:41], %[k]\n\t"
        "s_add_u32 s40, s40, s91\n\t"
        "s_addc_u32 s41, s41, 0\n\t"
        "s_and_b32 s41, s41, 0xffff\n\t"
        "s_sub_u32 s42, %[kvb], s91\n\t"
        "s_mov_b32 s43, 0x20000\n\t"
        "s_mov_b64 s[44:45], %[v]\n\t"
        "s_add_u32 s44, s44, s91\n\t"
        "s_addc_u32 s45, s45, 0\n\t"
        "s_and_b32 s45, s45, 0xffff\n\t"
        "s_sub_u32 s46, %[kvb], s91\n\t"
        "s_mov_b32 s47, 0x20000\n\t"
        "v_bfe_u32 v160, v238, 2, 1\n\t"
        "v_bfe_u32 v161, v238, 3, 1\n\t"
        "v_xor_b32 v162, v160, v161\n\t"
        "v_lshl_or_b32 v162, v162, 1, v161\n\t"
        "v_lshrrev_b32 v163, 4, v238\n\t"
        "v_xor_b32 v162, v162, v163\n\t"
        "v_lshlrev_b32 v162, 4, v162\n\t"
        "v_and_b32 v164, 7, v238\n\t"
        "v_lshl_add_u32 v162, v164, 6, v162\n\t"
        "v_lshl_add_u32 v162, v161, 12, v162\n\t"
        "s_lshl_b32 s88, s68, 14\n\t"
        "s_add_u32 s88, s88, %[lds]\n\t"
        "v_add_u32 v232, s88, v162\n\t"
        "s_lshl_b32 s89, s69, 15\n\t"
        "s_add_u32 s89, s89, %[lds]\n\t"
        "s_add_u32 s89, s89, 0x18000\n\t"
        "v_add_u32 v168, s89, v162\n\t"
        "v_bfe_u32 v160, v238, 4, 1\n\t"
        "v_lshrrev_b32 v161, 5, v238\n\t"
        "v_xor_b32 v162, v160, v161\n\t"
        "v_lshl_or_b32 v162, v162, 1, v161\n\t"
        "v_bfe_u32 v163, v238, 1, 1\n\t"
        "v_xor_b32 v162, v162, v163\n\t"
        "v_lshlrev_b32 v162, 4, v162\n\t"
        "v_bfe_u32 v163, v238, 2, 3\n\t"
        "v_lshl_add_u32 v162, v163, 6, v162\n\t"
        "v_lshl_add_u32 v162, v161, 12, v162\n\t"
        "v_and_b32 v163, 1, v238\n\t"
        "v_lshl_add_u32 v162, v163, 3, v162\n\t"
        "s_add_u32 s88, s88, 0x10000\n\t"
        "v_add_u32 v233, s88, v162\n\t"
        "s_lshl_b32 s89, s68, 11\n\t"
        "v_subrev_u32 v251, s89, v233\n\t"
        "v_add_u32 v233, s89, v233\n\t"
        "v_xor_b32 v234, 32, v233\n\t"
        "v_xor_b32 v252, 32, v251\n\t"
        "s_mov_b32 s56, 0x3db8aa3b\n\t"
        "s_mov_b32 s63, 0x45800000\n\t"
        "s_mov_b32 s67, 0\n\t"
        "s_waitcnt lgkmcnt(0)\n\t"
        "s_min_i32 s59, s59, 2048\n\t"
        "s_max_i32 s59, s59, 0\n\t"
        "s_add_u32 s57, s59, 63\n\t"
        "s_lshr_b32 s57, s57, 6\n\t"
        "s_sub_u32 s65, s57, 1\n\t"
        "s_cmp_lt_u32 s57, 2\n\t"
        "s_cbranch_scc1 SLOWALL_%=\n\t"
        "s_waitcnt vmcnt(9)\n\t"
        "v_lshlrev_b32 v237, 16, v237\n\t"
        "v_cmp_gt_u32 vcc, s57, v238\n\t"
        "s_nop 1\n\t"
        "v_cndmask_b32 v237, 0, v237, vcc\n\t"
        "s_nop 0\n\t"
        "v_readlane_b32 s60, v237, 0\n\t"
        "v_readlane_b32 s61, v237, 1\n\t"
        "s_lshr_b32 s88, %[blk], 3\n\t"
        "s_and_b32 s89, s88, 15\n\t"
        "s_lshl_b32 s89, s89, 12\n\t"
        "v_lshlrev_b32 v248, 3, v238\n\t"
        "v_add_u32 v248, s89, v248\n\t"
        "s_bitcmp1_b32 %[blk], 7\n\t"
        "s_cselect_b64 s[76:77], s[44:45], s[40:41]\n\t"
        "s_cselect_b64 s[78:79], s[46:47], s[42:43]\n\t"
        "v_readlane_b32 s88, v237, 0\n\t"
        "v_readlane_b32 s89, v237, 1\n\t"
        "v_readlane_b32 s90, v237, 2\n\t"
        "v_readlane_b32 s91, v237, 3\n\t"
        "s_mov_b32 s80, 4\n\t"
        "s_nop 4\n\t"
        "s_add_u32 m0, s62, 0x0\n\t"
        "v_accvgpr_write_b32 a32, 0\n\t"
        "buffer_load_dwordx4 v235, s[40:43], s60 offen offset:0 lds\n\t"
        "v_accvgpr_write_b32 a33, 0\n\t"
        "v_accvgpr_write_b32 a34, 0\n\t"
        "v_accvgpr_write_b32 a35, 0\n\t"
        "s_add_u32 m0, s62, 0x1000\n\t"
        "v_accvgpr_write_b32 a36, 0\n\t"
        "buffer_load_dwordx4 v236, s[40:43], s60 offen offset:0 lds\n\t"
        "v_accvgpr_write_b32 a37, 0\n\t"
        "v_accvgpr_write_b32 a38, 0\n\t"
        "v_accvgpr_write_b32 a39, 0\n\t"
        "s_add_u32 m0, s62, 0x380\n\t"
        "v_accvgpr_write_b32 a40, 0\n\t"
        "buffer_load_dwordx4 v235, s[40:43], s60 offen offset:128 lds\n\t"
        "v_accvgpr_write_b32 a41, 0\n\t"
        "v_accvgpr_write_b32 a42, 0\n\t"
        "v_accvgpr_write_b32 a43, 0\n\t"
        "s_add_u32 m0, s62, 0x1380\n\t"
        "v_accvgpr_write_b32 a44, 0\n\t"
        "buffer_load_dwordx4 v236, s[40:43], s60 offen offset:128 lds\n\t"
        "v_accvgpr_write_b32 a45, 0\n\t"
        "v_accvgpr_write_b32 a46, 0\n\t"
        "v_accvgpr_write_b32 a47, 0\n\t"
        "s_add_u32 m0, s62, 0x700\n\t"
        "v_accvgpr_write_b32 a48, 0\n\t"
        "buffer_load_dwordx4 v235, s[40:43], s60 offen offset:256 lds\n\t"
        "v_accvgpr_write_b32 a49, 0\n\t"
        "v_accvgpr_write_b32 a50, 0\n\t"
        "v_accvgpr_write_b32 a51, 0\n\t"
        "s_add_u32 m0, s62, 0x1700\n\t"
        "v_accvgpr_write_b32 a52, 0\n\t"
        "buffer_load_dwordx4 v236, s[40:43], s60 offen offset:256 lds\n\t"
        "v_accvgpr_write_b32 a53, 0\n\t"
        "v_accvgpr_write_b32 a54, 0\n\t"
        "v_accvgpr_write_b32 a55, 0\n\t"
        "s_add_u32 m0, s62, 0xa80\n\t"
        "v_accvgpr_write_b32 a56, 0\n\t"
        "buffer_load_dwordx4 v235, s[40:43], s60 offen offset:384 lds\n\t"
        "v_accvgpr_write_b32 a57, 0\n\t"
        "v_accvgpr_write_b32 a58, 0\n\t"
        "v_accvgpr_write_b32 a59, 0\n\t"
        "s_add_u32 m0, s62, 0x1a80\n\t"
        "v_accvgpr_write_b32 a60, 0\n\t"
        "buffer_load_dwordx4 v236, s[40:43], s60 offen offset:384 lds\n\t"
        "v_accvgpr_write_b32 a61, 0\n\t"
        "v_accvgpr_write_b32 a62, 0\n\t"
        "v_accvgpr_write_b32 a63, 0\n\t"
        "s_add_u32 m0, s85, 0x18700\n\t"
        "v_accvgpr_write_b32 a64, 0\n\t"
        "buffer_load_dwordx4 v166, s[48:51], s86 offen offset:256 nt lds\n\t"
        "v_accvgpr_write_b32 a65, 0\n\t"
        "v_accvgpr_write_b32 a66, 0\n\t"
        "v_accvgpr_write_b32 a67, 0\n\t"
        "s_add_u32 m0, s85, 0x19700\n\t"
        "v_accvgpr_write_b32 a68, 0\n\t"
        "buffer_load_dwordx4 v167, s[48:51], s86 offen offset:256 nt lds\n\t"
        "v_accvgpr_write_b32 a69, 0\n\t"
        "v_accvgpr_write_b32 a70, 0\n\t"
        "v_accvgpr_write_b32 a71, 0\n\t"
        "s_add_u32 m0, s85, 0x1a700\n\t"
        "v_accvgpr_write_b32 a72, 0\n\t"
        "buffer_load_dwordx4 v166, s[48:51], s87 offen offset:256 nt lds\n\t"
        "v_accvgpr_write_b32 a73, 0\n\t"
        "v_accvgpr_write_b32 a74, 0\n\t"
        "v_accvgpr_write_b32 a75, 0\n\t"
        "s_add_u32 m0, s85, 0x1b700\n\t"
        "v_accvgpr_write_b32 a76, 0\n\t"
        "buffer_load_dwordx4 v167, s[48:51], s87 offen offset:256 nt lds\n\t"
        "v_accvgpr_write_b32 a77, 0\n\t"
        "v_accvgpr_write_b32 a78, 0\n\t"
        "v_accvgpr_write_b32 a79, 0\n\t"
        "buffer_load_dword v249, v248, s[76:79], s88 offen\n\t"
        "buffer_load_dword v249, v248, s[76:79], s89 offen\n\t"
        "buffer_load_dword v249, v248, s[76:79], s90 offen\n\t"
        "buffer_load_dword v249, v248, s[76:79], s91 offen\n\t"
        "s_add_u32 m0, s85, 0x18a80\n\t"
        "v_accvgpr_write_b32 a80, 0\n\t"
        "buffer_load_dwordx4 v166, s[48:51], s86 offen offset:384 nt lds\n\t"
        "v_accvgpr_write_b32 a81, 0\n\t"
        "v_accvgpr_write_b32 a82, 0\n\t"
        "v_accvgpr_write_b32 a83, 0\n\t"
        "s_add_u32 m0, s85, 0x19a80\n\t"
        "v_accvgpr_write_b32 a84, 0\n\t"
        "buffer_load_dwordx4 v167, s[48:51], s86 offen offset:384 nt lds\n\t"
        "v_accvgpr_write_b32 a85, 0\n\t"
        "v_accvgpr_write_b32 a86, 0\n\t"
        "v_accvgpr_write_b32 a87, 0\n\t"
        "s_add_u32 m0, s85, 0x1aa80\n\t"
        "v_accvgpr_write_b32 a88, 0\n\t"
        "buffer_load_dwordx4 v166, s[48:51], s87 offen offset:384 nt lds\n\t"
        "v_accvgpr_write_b32 a89, 0\n\t"
        "v_accvgpr_write_b32 a90, 0\n\t"
        "v_accvgpr_write_b32 a91, 0\n\t"
        "s_add_u32 m0, s85, 0x1ba80\n\t"
        "v_accvgpr_write_b32 a92, 0\n\t"
        "buffer_load_dwordx4 v167, s[48:51], s87 offen offset:384 nt lds\n\t"
        "v_accvgpr_write_b32 a93, 0\n\t"
        "v_accvgpr_write_b32 a94, 0\n\t"
        "v_accvgpr_write_b32 a95, 0\n\t"
        "v_accvgpr_write_b32 a96, 0\n\t"
        "v_accvgpr_write_b32 a97, 0\n\t"
        "v_accvgpr_write_b32 a98, 0\n\t"
        "v_accvgpr_write_b32 a99, 0\n\t"
        "v_accvgpr_write_b32 a100, 0\n\t"
        "v_accvgpr_write_b32 a101, 0\n\t"
        "v_accvgpr_write_b32 a102, 0\n\t"
        "v_accvgpr_write_b32 a103, 0\n\t"
        "v_accvgpr_write_b32 a104, 0\n\t"
        "v_accvgpr_write_b32 a105, 0\n\t"
        "v_accvgpr_write_b32 a106, 0\n\t"
        "v_accvgpr_write_b32 a107, 0\n\t"
        "v_accvgpr_write_b32 a108, 0\n\t"
        "v_accvgpr_write_b32 a109, 0\n\t"
        "v_accvgpr_write_b32 a110, 0\n\t"
        "v_accvgpr_write_b32 a111, 0\n\t"
        "v_accvgpr_write_b32 a112, 0\n\t"
        "v_accvgpr_write_b32 a113, 0\n\t"
        "v_accvgpr_write_b32 a114, 0\n\t"
        "v_accvgpr_write_b32 a115, 0\n\t"
        "v_accvgpr_write_b32 a116, 0\n\t"
        "v_accvgpr_write_b32 a117, 0\n\t"
        "v_accvgpr_write_b32 a118, 0\n\t"
        "v_accvgpr_write_b32 a119, 0\n\t"
        "v_accvgpr_write_b32 a120, 0\n\t"
        "v_accvgpr_write_b32 a121, 0\n\t"
        "v_accvgpr_write_b32 a122, 0\n\t"
        "v_accvgpr_write_b32 a123, 0\n\t"
        "v_accvgpr_write_b32 a124, 0\n\t"
        "v_accvgpr_write_b32 a125, 0\n\t"
        "v_accvgpr_write_b32 a126, 0\n\t"
        "v_accvgpr_write_b32 a127, 0\n\t"
        "v_accvgpr_write_b32 a128, 0\n\t"
        "v_accvgpr_write_b32 a129, 0\n\t"
        "v_accvgpr_write_b32 a130, 0\n\t"
        "v_accvgpr_write_b32 a131, 0\n\t"
        "v_accvgpr_write_b32 a132, 0\n\t"
        "v_accvgpr_write_b32 a133, 0\n\t"
        "v_accvgpr_write_b32 a134, 0\n\t"
        "v_accvgpr_write_b32 a135, 0\n\t"
        "v_accvgpr_write_b32 a136, 0\n\t"
        "v_accvgpr_write_b32 a137, 0\n\t"
        "v_accvgpr_write_b32 a138, 0\n\t"
        "v_accvgpr_write_b32 a139, 0\n\t"
        "v_accvgpr_write_b32 a140, 0\n\t"
        "v_accvgpr_write_b32 a141, 0\n\t"
        "v_accvgpr_write_b32 a142, 0\n\t"
        "v_accvgpr_write_b32 a143, 0\n\t"
        "v_accvgpr_write_b32 a144, 0\n\t"
        "v_accvgpr_write_b32 a145, 0\n\t"
        "v_accvgpr_write_b32 a146, 0\n\t"
        "v_accvgpr_write_b32 a147, 0\n\t"
        "v_accvgpr_write_b32 a148, 0\n\t"
        "v_accvgpr_write_b32 a149, 0\n\t"
        "v_accvgpr_write_b32 a150, 0\n\t"
        "v_accvgpr_write_b32 a151, 0\n\t"
        "v_accvgpr_write_b32 a152, 0\n\t"
        "v_accvgpr_write_b32 a153, 0\n\t"
        "v_accvgpr_write_b32 a154, 0\n\t"
        "v_accvgpr_write_b32 a155, 0\n\t"
        "v_accvgpr_write_b32 a156, 0\n\t"
        "v_accvgpr_write_b32 a157, 0\n\t"
        "v_accvgpr_write_b32 a158, 0\n\t"
        "v_accvgpr_write_b32 a159, 0\n\t"
        "v_accvgpr_write_b32 a160, 0\n\t"
        "v_accvgpr_write_b32 a161, 0\n\t"
        "v_accvgpr_write_b32 a162, 0\n\t"
        "v_accvgpr_write_b32 a163, 0\n\t"
        "v_accvgpr_write_b32 a164, 0\n\t"
        "v_accvgpr_write_b32 a165, 0\n\t"
        "v_accvgpr_write_b32 a166, 0\n\t"
        "v_accvgpr_write_b32 a167, 0\n\t"
        "v_accvgpr_write_b32 a168, 0\n\t"
        "v_accvgpr_write_b32 a169, 0\n\t"
        "v_accvgpr_write_b32 a170, 0\n\t"
        "v_accvgpr_write_b32 a171, 0\n\t"
        "v_accvgpr_write_b32 a172, 0\n\t"
        "v_accvgpr_write_b32 a173, 0\n\t"
        "v_accvgpr_write_b32 a174, 0\n\t"
        "v_accvgpr_write_b32 a175, 0\n\t"
        "v_accvgpr_write_b32 a176, 0\n\t"
        "v_accvgpr_write_b32 a177, 0\n\t"
        "v_accvgpr_write_b32 a178, 0\n\t"
        "v_accvgpr_write_b32 a179, 0\n\t"
        "v_accvgpr_write_b32 a180, 0\n\t"
        "v_accvgpr_write_b32 a181, 0\n\t"
        "v_accvgpr_write_b32 a182, 0\n\t"
        "v_accvgpr_write_b32 a183, 0\n\t"
        "v_accvgpr_write_b32 a184, 0\n\t"
        "v_accvgpr_write_b32 a185, 0\n\t"
        "v_accvgpr_write_b32 a186, 0\n\t"
        "v_accvgpr_write_b32 a187, 0\n\t"
        "v_accvgpr_write_b32 a188, 0\n\t"
        "v_accvgpr_write_b32 a189, 0\n\t"
        "v_accvgpr_write_b32 a190, 0\n\t"
        "v_accvgpr_write_b32 a191, 0\n\t"
        "v_accvgpr_write_b32 a192, 0\n\t"
        "v_accvgpr_write_b32 a193, 0\n\t"
        "v_accvgpr_write_b32 a194, 0\n\t"
        "v_accvgpr_write_b32 a195, 0\n\t"
        "v_accvgpr_write_b32 a196, 0\n\t"
        "v_accvgpr_write_b32 a197, 0\n\t"
        "v_accvgpr_write_b32 a198, 0\n\t"
        "v_accvgpr_write_b32 a199, 0\n\t"
        "v_accvgpr_write_b32 a200, 0\n\t"
        "v_accvgpr_write_b32 a201, 0\n\t"
        "v_accvgpr_write_b32 a202, 0\n\t"
        "v_accvgpr_write_b32 a203, 0\n\t"
        "v_accvgpr_write_b32 a204, 0\n\t"
        "v_accvgpr_write_b32 a205, 0\n\t"
        "v_accvgpr_write_b32 a206, 0\n\t"
        "v_accvgpr_write_b32 a207, 0\n\t"
        "v_accvgpr_write_b32 a208, 0\n\t"
        "v_accvgpr_write_b32 a209, 0\n\t"
        "v_accvgpr_write_b32 a210, 0\n\t"
        "v_accvgpr_write_b32 a211, 0\n\t"
        "v_accvgpr_write_b32 a212, 0\n\t"
        "v_accvgpr_write_b32 a213, 0\n\t"
        "v_accvgpr_write_b32 a214, 0\n\t"
        "v_accvgpr_write_b32 a215, 0\n\t"
        "v_accvgpr_write_b32 a216, 0\n\t"
        "v_accvgpr_write_b32 a217, 0\n\t"
        "v_accvgpr_write_b32 a218, 0\n\t"
        "v_accvgpr_write_b32 a219, 0\n\t"
        "v_accvgpr_write_b32 a220, 0\n\t"
        "v_accvgpr_write_b32 a221, 0\n\t"
        "v_accvgpr_write_b32 a222, 0\n\t"
        "v_accvgpr_write_b32 a223, 0\n\t"
        "v_accvgpr_write_b32 a224, 0\n\t"
        "v_accvgpr_write_b32 a225, 0\n\t"
        "v_accvgpr_write_b32 a226, 0\n\t"
        "v_accvgpr_write_b32 a227, 0\n\t"
        "v_accvgpr_write_b32 a228, 0\n\t"
        "v_accvgpr_write_b32 a229, 0\n\t"
        "v_accvgpr_write_b32 a230, 0\n\t"
        "v_accvgpr_write_b32 a231, 0\n\t"
        "v_accvgpr_write_b32 a232, 0\n\t"
        "v_accvgpr_write_b32 a233, 0\n\t"
        "v_accvgpr_write_b32 a234, 0\n\t"
        "v_accvgpr_write_b32 a235, 0\n\t"
        "v_accvgpr_write_b32 a236, 0\n\t"
        "v_accvgpr_write_b32 a237, 0\n\t"
        "v_accvgpr_write_b32 a238, 0\n\t"
        "v_accvgpr_write_b32 a239, 0\n\t"
        "v_accvgpr_write_b32 a240, 0\n\t"
        "v_accvgpr_write_b32 a241, 0\n\t"
        "v_accvgpr_write_b32 a242, 0\n\t"
        "v_accvgpr_write_b32 a243, 0\n\t"
        "v_accvgpr_write_b32 a244, 0\n\t"
        "v_accvgpr_write_b32 a245, 0\n\t"
        "v_accvgpr_write_b32 a246, 0\n\t"
        "v_accvgpr_write_b32 a247, 0\n\t"
        "v_accvgpr_write_b32 a248, 0\n\t"
        "v_accvgpr_write_b32 a249, 0\n\t"
        "v_accvgpr_write_b32 a250, 0\n\t"
        "v_accvgpr_write_b32 a251, 0\n\t"
        "v_accvgpr_write_b32 a252, 0\n\t"
        "v_accvgpr_write_b32 a253, 0\n\t"
        "v_accvgpr_write_b32 a254, 0\n\t"
        "v_accvgpr_write_b32 a255, 0\n\t"
        "v_mov_b32 v224, 0\n\t"
        "v_mov_b32 v226, 0\n\t"
        "v_mov_b32 v228, 0\n\t"
        "v_mov_b32 v230, 0\n\t"
        "v_mov_b32 v240, 0\n\t"
        "v_mov_b32 v241, 0\n\t"
        "v_mov_b32 v242, 0\n\t"
        "v_mov_b32 v243, 0\n\t"
        "v_mov_b32 v244, 0\n\t"
        "v_mov_b32 v245, 0\n\t"
        "v_mov_b32 v246, 0\n\t"
        "v_mov_b32 v247, 0\n\t"
        "s_waitcnt vmcnt(18)\n\t"
        "s_barrier\n\t"
        "ds_read_b128 v[208:211], v232 offset:0\n\t"
        "ds_read_b128 v[0:3], v168 offset:0\n\t"
        "ds_read_b128 v[32:35], v168 offset:8192\n\t"
        "ds_read_b128 v[64:67], v168 offset:16384\n\t"
        "ds_read_b128 v[96:99], v168 offset:24576\n\t"
        "ds_read_b128 v[212:215], v232 offset:8192\n\t"
        "ds_read_b128 v[216:219], v232 offset:512\n\t"
        "ds_read_b128 v[4:7], v168 offset:512\n\t"
        "ds_read_b128 v[36:39], v168 offset:8704\n\t"
        "ds_read_b128 v[68:71], v168 offset:16896\n\t"
        "ds_read_b128 v[100:103], v168 offset:25088\n\t"
        "ds_read_b128 v[220:223], v232 offset:8704\n\t"
        "s_waitcnt lgkmcnt(7)\n\t"
        "v_mfma_f32_16x16x32_bf16 v[128:131], v[208:211], v[0:3], 0\n\t"
        "s_add_u32 m0, s62, 0x8000\n\t"
        "v_mfma_f32_16x16x32_bf16 v[132:135], v[208:211], v[32:35], 0\n\t"
        "buffer_load_dwordx4 v235, s[40:43], s61 offen offset:0 lds\n\t"
        "v_mfma_f32_16x16x32_bf16 v[136:139], v[208:211], v[64:67], 0\n\t"
        "v_mfma_f32_16x16x32_bf16 v[140:143], v[208:211], v[96:99], 0\n\t"
        "s_waitcnt lgkmcnt(6)\n\t"
        "v_mfma_f32_16x16x32_bf16 v[144:147], v[212:215], v[0:3], 0\n\t"
        "s_add_u32 m0, s62, 0x8380\n\t"
        "v_mfma_f32_16x16x32_bf16 v[148:151], v[212:215], v[32:35], 0\n\t"
        "buffer_load_dwordx4 v235, s[40:43], s61 offen offset:128 lds\n\t"
        "v_mfma_f32_16x16x32_bf16 v[152:155], v[212:215], v[64:67], 0\n\t"
        "v_mfma_f32_16x16x32_bf16 v[156:159], v[212:215], v[96:99], 0\n\t"
        "s_waitcnt lgkmcnt(1)\n\t"
        "v_mfma_f32_16x16x32_bf16 v[128:131], v[216:219], v[4:7], v[128:131]\n\t"
        "s_add_u32 m0, s62, 0x8700\n\t"
        "v_mfma_f32_16x16x32_bf16 v[132:135], v[216:219], v[36:39], v[132:135]\n\t"
        "buffer_load_dwordx4 v235, s[40:43], s61 offen offset:256 lds\n\t"
        "v_mfma_f32_16x16x32_bf16 v[136:139], v[216:219], v[68:71], v[136:139]\n\t"
        "v_mfma_f32_16x16x32_bf16 v[140:143], v[216:219], v[100:103], v[140:143]\n\t"
        "s_waitcnt lgkmcnt(0)\n\t"
        "v_mfma_f32_16x16x32_bf16 v[144:147], v[220:223], v[4:7], v[144:147]\n\t"
        "s_add_u32 m0, s62, 0x8a80\n\t"
        "v_mfma_f32_16x16x32_bf16 v[148:151], v[220:223], v[36:39], v[148:151]\n\t"
        "buffer_load_dwordx4 v235, s[40:43], s61 offen offset:384 lds\n\t"
        "v_mfma_f32_16x16x32_bf16 v[152:155], v[220:223], v[68:71], v[152:155]\n\t"
        "v_mfma_f32_16x16x32_bf16 v[156:159], v[220:223], v[100:103], v[156:159]\n\t"
        "s_waitcnt vmcnt(20)\n\t"
        "s_barrier\n\t"
        "ds_read_b128 v[208:211], v232 offset:1024\n\t"
        "ds_read_b128 v[8:11], v168 offset:1024\n\t"
        "ds_read_b128 v[40:43], v168 offset:9216\n\t"
        "ds_read_b128 v[72:75], v168 offset:17408\n\t"
        "ds_read_b128 v[104:107], v168 offset:25600\n\t"
        "ds_read_b128 v[212:215], v232 offset:9216\n\t"
        "ds_read_b128 v[216:219], v232 offset:1536\n\t"
        "ds_read_b128 v[12:15], v168 offset:1536\n\t"
        "ds_read_b128 v[44:47], v168 offset:9728\n\t"
        "ds_read_b128 v[76:79], v168 offset:17920\n\t"
        "ds_read_b128 v[108:111], v168 offset:26112\n\t"
        "ds_read_b128 v[220:223], v232 offset:9728\n\t"
        "s_waitcnt lgkmcnt(7)\n\t"
        "v_mfma_f32_16x16x32_bf16 v[128:131], v[208:211], v[8:11], v[128:131]\n\t"
        "s_add_u32 m0, s62, 0x9000\n\t"
        "v_mfma_f32_16x16x32_bf16 v[132:135], v[208:211], v[40:43], v[132:135]\n\t"
        "buffer_load_dwordx4 v236, s[40:43], s61 offen offset:0 lds\n\t"
        "v_mfma_f32_16x16x32_bf16 v[136:139], v[208:211], v[72:75], v[136:139]\n\t"
        "v_mfma_f32_16x16x32_bf16 v[140:143], v[208:211], v[104:107], v[140:143]\n\t"
        "s_waitcnt lgkmcnt(6)\n\t"
        "v_mfma_f32_16x16x32_bf16 v[144:147], v[212:215], v[8:11], v[144:147]\n\t"
        "s_add_u32 m0, s62, 0x9380\n\t"
        "v_mfma_f32_16x16x32_bf16 v[148:151], v[212:215], v[40:43], v[148:151]\n\t"
        "buffer_load_dwordx4 v236, s[40:43], s61 offen offset:128 lds\n\t"
        "v_mfma_f32_16x16x32_bf16 v[152:155], v[212:215], v[72:75], v[152:155]\n\t"
        "v_mfma_f32_16x16x32_bf16 v[156:159], v[212:215], v[104:107], v[156:159]\n\t"
        "s_waitcnt lgkmcnt(1)\n\t"
        "v_mfma_f32_16x16x32_bf16 v[128:131], v[216:219], v[12:15], v[128:131]\n\t"
        "s_add_u32 m0, s62, 0x9700\n\t"
        "v_mfma_f32_16x16x32_bf16 v[132:135], v[216:219], v[44:47], v[132:135]\n\t"
        "buffer_load_dwordx4 v236, s[40:43], s61 offen offset:256 lds\n\t"
        "v_mfma_f32_16x16x32_bf16 v[136:139], v[216:219], v[76:79], v[136:139]\n\t"
        "v_mfma_f32_16x16x32_bf16 v[140:143], v[216:219], v[108:111], v[140:143]\n\t"
        "s_waitcnt lgkmcnt(0)\n\t"
        "v_mfma_f32_16x16x32_bf16 v[144:147], v[220:223], v[12:15], v[144:147]\n\t"
        "s_add_u32 m0, s62, 0x9a80\n\t"
        "v_mfma_f32_16x16x32_bf16 v[148:151], v[220:223], v[44:47], v[148:151]\n\t"
        "buffer_load_dwordx4 v236, s[40:43], s61 offen offset:384 lds\n\t"
        "v_mfma_f32_16x16x32_bf16 v[152:155], v[220:223], v[76:79], v[152:155]\n\t"
        "v_mfma_f32_16x16x32_bf16 v[156:159], v[220:223], v[108:111], v[156:159]\n\t"
        "s_waitcnt vmcnt(16)\n\t"
        "s_barrier\n\t"
        "ds_read_b128 v[208:211], v232 offset:2048\n\t"
        "ds_read_b128 v[16:19], v168 offset:2048\n\t"
        "ds_read_b128 v[48:51], v168 offset:10240\n\t"
        "ds_read_b128 v[80:83], v168 offset:18432\n\t"
        "ds_read_b128 v[112:115], v168 offset:26624\n\t"
        "ds_read_b128 v[212:215], v232 offset:10240\n\t"
        "ds_read_b128 v[216:219], v232 offset:2560\n\t"
        "ds_read_b128 v[20:23], v168 offset:2560\n\t"
        "ds_read_b128 v[52:55], v168 offset:10752\n\t"
        "ds_read_b128 v[84:87], v168 offset:18944\n\t"
        "ds_read_b128 v[116:119], v168 offset:27136\n\t"
        "ds_read_b128 v[220:223], v232 offset:10752\n\t"
        "s_waitcnt lgkmcnt(7)\n\t"
        "v_mfma_f32_16x16x32_bf16 v[128:131], v[208:211], v[16:19], v[128:131]\n\t"
        "s_add_u32 m0, s62, 0x10000\n\t"
        "v_mfma_f32_16x16x32_bf16 v[132:135], v[208:211], v[48:51], v[132:135]\n\t"
        "buffer_load_dwordx4 v235, s[44:47], s60 offen offset:0 lds\n\t"
        "v_mfma_f32_16x16x32_bf16 v[136:139], v[208:211], v[80:83], v[136:139]\n\t"
        "v_mfma_f32_16x16x32_bf16 v[140:143], v[208:211], v[112:115], v[140:143]\n\t"
        "s_waitcnt lgkmcnt(6)\n\t"
        "v_mfma_f32_16x16x32_bf16 v[144:147], v[212:215], v[16:19], v[144:147]\n\t"
        "s_add_u32 m0, s62, 0x10380\n\t"
        "v_mfma_f32_16x16x32_bf16 v[148:151], v[212:215], v[48:51], v[148:151]\n\t"
        "buffer_load_dwordx4 v235, s[44:47], s60 offen offset:128 lds\n\t"
        "v_mfma_f32_16x16x32_bf16 v[152:155], v[212:215], v[80:83], v[152:155]\n\t"
        "v_mfma_f32_16x16x32_bf16 v[156:159], v[212:215], v[112:115], v[156:159]\n\t"
        "s_waitcnt lgkmcnt(1)\n\t"
        "v_mfma_f32_16x16x32_bf16 v[128:131], v[216:219], v[20:23], v[128:131]\n\t"
        "s_add_u32 m0, s62, 0x10700\n\t"
        "v_mfma_f32_16x16x32_bf16 v[132:135], v[216:219], v[52:55], v[132:135]\n\t"
        "buffer_load_dwordx4 v235, s[44:47], s60 offen offset:256 lds\n\t"
        "v_mfma_f32_16x16x32_bf16 v[136:139], v[216:219], v[84:87], v[136:139]\n\t"
        "v_mfma_f32_16x16x32_bf16 v[140:143], v[216:219], v[116:119], v[140:143]\n\t"
        "s_waitcnt lgkmcnt(0)\n\t"
        "v_mfma_f32_16x16x32_bf16 v[144:147], v[220:223], v[20:23], v[144:147]\n\t"
        "s_add_u32 m0, s62, 0x10a80\n\t"
        "v_mfma_f32_16x16x32_bf16 v[148:151], v[220:223], v[52:55], v[148:151]\n\t"
        "buffer_load_dwordx4 v235, s[44:47], s60 offen offset:384 lds\n\t"
        "v_mfma_f32_16x16x32_bf16 v[152:155], v[220:223], v[84:87], v[152:155]\n\t"
        "v_mfma_f32_16x16x32_bf16 v[156:159], v[220:223], v[116:119], v[156:159]\n\t"
        "s_waitcnt vmcnt(12)\n\t"
        "s_barrier\n\t"
        "ds_read_b128 v[208:211], v232 offset:3072\n\t"
        "ds_read_b128 v[24:27], v168 offset:3072\n\t"
        "ds_read_b128 v[56:59], v168 offset:11264\n\t"
        "ds_read_b128 v[88:91], v168 offset:19456\n\t"
        "ds_read_b128 v[120:123], v168 offset:27648\n\t"
        "ds_read_b128 v[212:215], v232 offset:11264\n\t"
        "ds_read_b128 v[216:219], v232 offset:3584\n\t"
        "ds_read_b128 v[28:31], v168 offset:3584\n\t"
        "ds_read_b128 v[60:63], v168 offset:11776\n\t"
        "ds_read_b128 v[92:95], v168 offset:19968\n\t"
        "ds_read_b128 v[124:127], v168 offset:28160\n\t"
        "ds_read_b128 v[220:223], v232 offset:11776\n\t"
        "s_waitcnt lgkmcnt(7)\n\t"
        "v_mfma_f32_16x16x32_bf16 v[128:131], v[208:211], v[24:27], v[128:131]\n\t"
        "s_add_u32 m0, s62, 0x11000\n\t"
        "v_mfma_f32_16x16x32_bf16 v[132:135], v[208:211], v[56:59], v[132:135]\n\t"
        "buffer_load_dwordx4 v236, s[44:47], s60 offen offset:0 lds\n\t"
        "v_mfma_f32_16x16x32_bf16 v[136:139], v[208:211], v[88:91], v[136:139]\n\t"
        "v_mfma_f32_16x16x32_bf16 v[140:143], v[208:211], v[120:123], v[140:143]\n\t"
        "s_waitcnt lgkmcnt(6)\n\t"
        "v_mfma_f32_16x16x32_bf16 v[144:147], v[212:215], v[24:27], v[144:147]\n\t"
        "s_add_u32 m0, s62, 0x11380\n\t"
        "v_mfma_f32_16x16x32_bf16 v[148:151], v[212:215], v[56:59], v[148:151]\n\t"
        "buffer_load_dwordx4 v236, s[44:47], s60 offen offset:128 lds\n\t"
        "v_mfma_f32_16x16x32_bf16 v[152:155], v[212:215], v[88:91], v[152:155]\n\t"
        "v_mfma_f32_16x16x32_bf16 v[156:159], v[212:215], v[120:123], v[156:159]\n\t"
        "s_waitcnt lgkmcnt(1)\n\t"
        "v_mfma_f32_16x16x32_bf16 v[128:131], v[216:219], v[28:31], v[128:131]\n\t"
        "s_add_u32 m0, s62, 0x11700\n\t"
        "v_mfma_f32_16x16x32_bf16 v[132:135], v[216:219], v[60:63], v[132:135]\n\t"
        "buffer_load_dwordx4 v236, s[44:47], s60 offen offset:256 lds\n\t"
        "v_mfma_f32_16x16x32_bf16 v[136:139], v[216:219], v[92:95], v[136:139]\n\t"
        "v_mfma_f32_16x16x32_bf16 v[140:143], v[216:219], v[124:127], v[140:143]\n\t"
        "s_waitcnt lgkmcnt(0)\n\t"
        "v_mfma_f32_16x16x32_bf16 v[144:147], v[220:223], v[28:31], v[144:147]\n\t"
        "s_add_u32 m0, s62, 0x11a80\n\t"
        "v_mfma_f32_16x16x32_bf16 v[148:151], v[220:223], v[60:63], v[148:151]\n\t"
        "buffer_load_dwordx4 v236, s[44:47], s60 offen offset:384 lds\n\t"
        "v_mfma_f32_16x16x32_bf16 v[152:155], v[220:223], v[92:95], v[152:155]\n\t"
        "v_mfma_f32_16x16x32_bf16 v[156:159], v[220:223], v[124:127], v[156:159]\n\t"
        "s_mov_b32 s64, 2\n\t"
        "v_readlane_b32 s60, v237, s64\n\t"
        "v_readlane_b32 s81, v237, s80\n\t"
        "s_waitcnt vmcnt(8)\n\t"
        "s_barrier\n\t"
        "ds_read_b128 v[208:211], v232 offset:32768\n\t"
        "ds_read_b128 v[212:215], v232 offset:33280\n\t"
        "ds_read_b128 v[216:219], v232 offset:33792\n\t"
        "buffer_load_dword v249, v248, s[76:79], s81 offen\n\t"
        "s_add_u32 s80, s80, 1\n\t"
        "s_add_u32 m0, s62, 0x0\n\t"
        "s_nop 0\n\t"
        "buffer_load_dwordx4 v235, s[40:43], s60 offen offset:0 lds\n\t"
        "s_add_u32 m0, s62, 0x380\n\t"
        "s_nop 0\n\t"
        "buffer_load_dwordx4 v235, s[40:43], s60 offen offset:128 lds\n\t"
        "s_add_u32 m0, s62, 0x700\n\t"
        "s_nop 0\n\t"
        "buffer_load_dwordx4 v235, s[40:43], s60 offen offset:256 lds\n\t"
        "v_max3_f32 v160, v128, v129, v130\n\t"
        "v_max3_f32 v164, v131, v144, v145\n\t"
        "v_max3_f32 v161, v132, v133, v134\n\t"
        "v_max3_f32 v165, v135, v148, v149\n\t"
        "v_max3_f32 v162, v136, v137, v138\n\t"
        "v_max3_f32 v166, v139, v152, v153\n\t"
        "v_max3_f32 v163, v140, v141, v142\n\t"
        "v_max3_f32 v167, v143, v156, v157\n\t"
        "v_max3_f32 v160, v160, v146, v147\n\t"
        "v_max3_f32 v161, v161, v150, v151\n\t"
        "v_max3_f32 v162, v162, v154, v155\n\t"
        "v_max3_f32 v163, v163, v158, v159\n\t"
        "v_max_f32 v160, v160, v164\n\t"
        "v_max_f32 v161, v161, v165\n\t"
        "v_max_f32 v162, v162, v166\n\t"
        "v_max_f32 v163, v163, v167\n\t"
        "v_mov_b32 v164, v160\n\t"
        "v_mov_b32 v165, v161\n\t"
        "v_mov_b32 v166, v162\n\t"
        "v_mov_b32 v167, v163\n\t"
        "s_nop 0\n\t"
        "v_permlane16_swap_b32 v160, v164\n\t"
        "v_permlane16_swap_b32 v161, v165\n\t"
        "v_permlane16_swap_b32 v162, v166\n\t"
        "v_permlane16_swap_b32 v163, v167\n\t"
        "s_nop 0\n\t"
        "v_max_f32 v160, v160, v164\n\t"
        "v_max_f32 v161, v161, v165\n\t"
        "v_max_f32 v162, v162, v166\n\t"
        "v_max_f32 v163, v163, v167\n\t"
        "v_mov_b32 v164, v160\n\t"
        "v_mov_b32 v165, v161\n\t"
        "v_mov_b32 v166, v162\n\t"
        "v_mov_b32 v167, v163\n\t"
        "s_nop 0\n\t"
        "v_permlane32_swap_b32 v160, v164\n\t"
        "v_permlane32_swap_b32 v161, v165\n\t"
        "v_permlane32_swap_b32 v162, v166\n\t"
        "v_permlane32_swap_b32 v163, v167\n\t"
        "s_nop 0\n\t"
        "v_max_f32 v160, v160, v164\n\t"
        "v_max_f32 v161, v161, v165\n\t"
        "v_max_f32 v162, v162, v166\n\t"
        "v_max_f32 v163, v163, v167\n\t"
        "v_mul_f32 v160, s56, v160\n\t"
        "v_mul_f32 v161, s56, v161\n\t"
        "v_mul_f32 v162, s56, v162\n\t"
        "v_mul_f32 v163, s56, v163\n\t"
        "v_ceil_f32 v160, v160\n\t"
        "v_ceil_f32 v161, v161\n\t"
        "v_ceil_f32 v162, v162\n\t"
        "v_ceil_f32 v163, v163\n\t"
        "v_sub_f32 v225, 0, v160\n\t"
        "v_sub_f32 v227, 0, v161\n\t"
        "v_sub_f32 v229, 0, v162\n\t"
        "v_sub_f32 v231, 0, v163\n\t"
        "v_max3_f32 v164, |v160|, |v161|, |v162|\n\t"
        "v_add_f32 v164, |v163|, v164\n\t"
        "v_add_f32 v164, |v160|, v164\n\t"
        "v_add_f32 v164, |v161|, v164\n\t"
        "v_add_f32 v164, |v162|, v164\n\t"
        "v_add_f32 v164, |v163|, v164\n\t"
        "v_mov_b32 v165, 0x4b000000\n\t"
        "v_cmp_nlt_f32 vcc, v164, v165\n\t"
        "s_nop 4\n\t"
        "s_cmp_lg_u64 vcc, 0\n\t"
        "s_cselect_b32 s71, 1, 0\n\t"
        "s_or_b32 s67, s67, s71\n\t"
        "v_fma_f32 v128, v128, s56, v225\n\t"
        "v_fma_f32 v129, v129, s56, v225\n\t"
        "v_fma_f32 v130, v130, s56, v225\n\t"
        "v_fma_f32 v131, v131, s56, v225\n\t"
        "v_fma_f32 v132, v132, s56, v227\n\t"
        "v_fma_f32 v133, v133, s56, v227\n\t"
        "v_fma_f32 v134, v134, s56, v227\n\t"
        "v_fma_f32 v135, v135, s56, v227\n\t"
        "v_fma_f32 v136, v136, s56, v229\n\t"
        "v_fma_f32 v137, v137, s56, v229\n\t"
        "v_fma_f32 v138, v138, s56, v229\n\t"
        "v_fma_f32 v139, v139, s56, v229\n\t"
        "v_fma_f32 v140, v140, s56, v231\n\t"
        "v_fma_f32 v141, v141, s56, v231\n\t"
        "v_fma_f32 v142, v142, s56, v231\n\t"
        "v_fma_f32 v143, v143, s56, v231\n\t"
        "v_fma_f32 v144, v144, s56, v225\n\t"
        "v_fma_f32 v145, v145, s56, v225\n\t"
        "v_fma_f32 v146, v146, s56, v225\n\t"
        "v_fma_f32 v147, v147, s56, v225\n\t"
        "v_fma_f32 v148, v148, s56, v227\n\t"
        "v_fma_f32 v149, v149, s56, v227\n\t"
        "v_fma_f32 v150, v150, s56, v227\n\t"
        "v_fma_f32 v151, v151, s56, v227\n\t"
        "v_fma_f32 v152, v152, s56, v229\n\t"
        "v_fma_f32 v153, v153, s56, v229\n\t"
        "v_fma_f32 v154, v154, s56, v229\n\t"
        "v_fma_f32 v155, v155, s56, v229\n\t"
        "v_exp_f32 v128, v128\n\t"
        "v_exp_f32 v129, v129\n\t"
        "v_exp_f32 v130, v130\n\t"
        "v_exp_f32 v131, v131\n\t"
        "v_exp_f32 v132, v132\n\t"
        "v_exp_f32 v133, v133\n\t"
        "v_exp_f32 v134, v134\n\t"
        "v_exp_f32 v135, v135\n\t"
        "v_exp_f32 v136, v136\n\t"
        "v_exp_f32 v137, v137\n\t"
        "v_exp_f32 v138, v138\n\t"
        "v_exp_f32 v139, v139\n\t"
        "v_exp_f32 v140, v140\n\t"
        "v_exp_f32 v141, v141\n\t"
        "v_exp_f32 v142, v142\n\t"
        "v_exp_f32 v143, v143\n\t"
        "s_nop 0\n\t"
        "v_add_f32 v224, v224, v128\n\t"
        "v_add_f32 v224, v224, v129\n\t"
        "v_add_f32 v224, v224, v130\n\t"
        "v_add_f32 v224, v224, v131\n\t"
        "v_add_f32 v226, v226, v132\n\t"
        "v_add_f32 v226, v226, v133\n\t"
        "v_add_f32 v226, v226, v134\n\t"
        "v_add_f32 v226, v226, v135\n\t"
        "v_add_f32 v228, v228, v136\n\t"
        "v_add_f32 v228, v228, v137\n\t"
        "v_add_f32 v228, v228, v138\n\t"
        "v_add_f32 v228, v228, v139\n\t"
        "v_add_f32 v230, v230, v140\n\t"
        "v_add_f32 v230, v230, v141\n\t"
        "v_add_f32 v230, v230, v142\n\t"
        "v_add_f32 v230, v230, v143\n\t"
        "s_mov_b32 s58, 1\n\t"
        "s_mov_b32 s64, 3\n\t"
        "s_cmp_lg_u32 s58, s65\n\t"
        "s_cbranch_scc1 NOB0_%=\n\t"
        "s_mov_b64 s[48:49], %[o]\n\t"
        "s_add_u32 s48, s48, s92\n\t"
        "s_addc_u32 s49, s49, 0\n\t"
        "s_and_b32 s49, s49, 0xffff\n\t"
        "buffer_load_dword v249, off, s[48:51], 0\n\t"
        "s_lshl_b32 s71, s58, 6\n\t"
        "s_sub_u32 s71, s59, s71\n\t"
        "s_lshl_b32 s88, s68, 5\n\t"
        "s_sub_u32 s71, s71, s88\n\t"
        "v_lshrrev_b32 v160, 4, v238\n\t"
        "v_lshlrev_b32 v160, 2, v160\n\t"
        "v_sub_u32 v160, s71, v160\n\t"
        "v_mov_b32 v161, 0xd06e6b28\n\t"
        "v_mov_b32 v162, 0\n\t"
        "v_cmp_lt_i32 vcc, 0, v160\n\t"
        "s_nop 1\n\t"
        "v_cndmask_b32 v240, v161, v162, vcc\n\t"
        "v_cmp_lt_i32 vcc, 1, v160\n\t"
        "s_nop 1\n\t"
        "v_cndmask_b32 v241, v161, v162, vcc\n\t"
        "v_cmp_lt_i32 vcc, 2, v160\n\t"
        "s_nop 1\n\t"
        "v_cndmask_b32 v242, v161, v162, vcc\n\t"
        "v_cmp_lt_i32 vcc, 3, v160\n\t"
        "s_nop 1\n\t"
        "v_cndmask_b32 v243, v161, v162, vcc\n\t"
        "v_cmp_lt_i32 vcc, 16, v160\n\t"
        "s_nop 1\n\t"
        "v_cndmask_b32 v244, v161, v162, vcc\n\t"
        "v_cmp_lt_i32 vcc, 17, v160\n\t"
        "s_nop 1\n\t"
        "v_cndmask_b32 v245, v161, v162, vcc\n\t"
        "v_cmp_lt_i32 vcc, 18, v160\n\t"
        "s_nop 1\n\t"
        "v_cndmask_b32 v246, v161, v162, vcc\n\t"
        "v_cmp_lt_i32 vcc, 19, v160\n\t"
        "s_nop 1\n\t"
        "v_cndmask_b32 v247, v161, v162, vcc\n\t"
        "s_nop 1\n\t"
        "NOB0_%=:\n\t"
        ".p2align 6\n\t"
        "LOOP_%=:\n\t"
        "s_waitcnt lgkmcnt(2)\n\t"
        "v_mfma_f32_16x16x32_bf16 v[160:163], v[208:211], v[0:3], v[240:243]\n\t"
        "ds_read_b128 v[220:223], v232 offset:34304\n\t"
        "v_cvt_pk_bf16_f32 v192, v128, v129\n\t"
        "v_mfma_f32_16x16x32_bf16 v[164:167], v[208:211], v[32:35], v[240:243]\n\t"
        "s_add_u32 m0, s62, 0xa80\n\t"
        "v_exp_f32 v144, v144\n\t"
        "v_mfma_f32_16x16x32_bf16 v[168:171], v[208:211], v[64:67], v[240:243]\n\t"
        "buffer_load_dwordx4 v235, s[40:43], s60 offen offset:384 lds\n\t"
        "v_mfma_f32_16x16x32_bf16 v[172:175], v[208:211], v[96:99], v[240:243]\n\t"
        "s_waitcnt lgkmcnt(2)\n\t"
        "v_add_f32 v224, v224, v144\n\t"
        "v_cvt_pk_bf16_f32 v193, v130, v131\n\t"
        "v_mfma_f32_16x16x32_bf16 v[160:163], v[212:215], v[4:7], v[160:163]\n\t"
        "ds_read_b128 v[208:211], v232 offset:34816\n\t"
        "v_cvt_pk_bf16_f32 v196, v132, v133\n\t"
        "v_mfma_f32_16x16x32_bf16 v[164:167], v[212:215], v[36:39], v[164:167]\n\t"
        "s_add_u32 m0, s62, 0x1000\n\t"
        "v_exp_f32 v145, v145\n\t"
        "v_mfma_f32_16x16x32_bf16 v[168:171], v[212:215], v[68:71], v[168:171]\n\t"
        "buffer_load_dwordx4 v236, s[40:43], s60 offen offset:0 lds\n\t"
        "v_mfma_f32_16x16x32_bf16 v[172:175], v[212:215], v[100:103], v[172:175]\n\t"
        "s_waitcnt lgkmcnt(2)\n\t"
        "v_cvt_pk_bf16_f32 v197, v134, v135\n\t"
        "v_cvt_pk_bf16_f32 v200, v136, v137\n\t"
        "v_mfma_f32_16x16x32_bf16 v[160:163], v[216:219], v[8:11], v[160:163]\n\t"
        "ds_read_b128 v[212:215], v232 offset:35328\n\t"
        "v_cvt_pk_bf16_f32 v201, v138, v139\n\t"
        "v_mfma_f32_16x16x32_bf16 v[164:167], v[216:219], v[40:43], v[164:167]\n\t"
        "s_add_u32 m0, s62, 0x1380\n\t"
        "v_exp_f32 v146, v146\n\t"
        "v_mfma_f32_16x16x32_bf16 v[168:171], v[216:219], v[72:75], v[168:171]\n\t"
        "buffer_load_dwordx4 v236, s[40:43], s60 offen offset:128 lds\n\t"
        "v_mfma_f32_16x16x32_bf16 v[172:175], v[216:219], v[104:107], v[172:175]\n\t"
        "s_waitcnt lgkmcnt(2)\n\t"
        "v_cvt_pk_bf16_f32 v204, v140, v141\n\t"
        "v_cvt_pk_bf16_f32 v205, v142, v143\n\t"
        "v_mfma_f32_16x16x32_bf16 v[160:163], v[220:223], v[12:15], v[160:163]\n\t"
        "ds_read_b128 v[216:219], v232 offset:35840\n\t"
        "v_add_f32 v224, v224, v145\n\t"
        "v_mfma_f32_16x16x32_bf16 v[164:167], v[220:223], v[44:47], v[164:167]\n\t"
        "s_add_u32 m0, s62, 0x1700\n\t"
        "v_exp_f32 v147, v147\n\t"
        "v_mfma_f32_16x16x32_bf16 v[168:171], v[220:223], v[76:79], v[168:171]\n\t"
        "buffer_load_dwordx4 v236, s[40:43], s60 offen offset:256 lds\n\t"
        "v_mfma_f32_16x16x32_bf16 v[172:175], v[220:223], v[108:111], v[172:175]\n\t"
        "s_waitcnt lgkmcnt(2)\n\t"
        "v_add_f32 v224, v224, v146\n\t"
        "v_cvt_pk_bf16_f32 v194, v144, v145\n\t"
        "v_mfma_f32_16x16x32_bf16 v[160:163], v[208:211], v[16:19], v[160:163]\n\t"
        "ds_read_b128 v[220:223], v232 offset:36352\n\t"
        "v_add_f32 v224, v224, v147\n\t"
        "v_mfma_f32_16x16x32_bf16 v[164:167], v[208:211], v[48:51], v[164:167]\n\t"
        "s_add_u32 m0, s62, 0x1a80\n\t"
        "v_exp_f32 v148, v148\n\t"
        "v_mfma_f32_16x16x32_bf16 v[168:171], v[208:211], v[80:83], v[168:171]\n\t"
        "buffer_load_dwordx4 v236, s[40:43], s60 offen offset:384 lds\n\t"
        "v_mfma_f32_16x16x32_bf16 v[172:175], v[208:211], v[112:115], v[172:175]\n\t"
        "s_waitcnt lgkmcnt(2)\n\t"
        "v_add_f32 v226, v226, v148\n\t"
        "v_cvt_pk_bf16_f32 v195, v146, v147\n\t"
        "v_mfma_f32_16x16x32_bf16 v[160:163], v[212:215], v[20:23], v[160:163]\n\t"
        "ds_read_b128 v[208:211], v232 offset:40960\n\t"
        "v_fma_f32 v156, v156, s56, v231\n\t"
        "v_mfma_f32_16x16x32_bf16 v[164:167], v[212:215], v[52:55], v[164:167]\n\t"
        "v_exp_f32 v149, v149\n\t"
        "v_mfma_f32_16x16x32_bf16 v[168:171], v[212:215], v[84:87], v[168:171]\n\t"
        "v_exp_f32 v150, v150\n\t"
        "v_mfma_f32_16x16x32_bf16 v[172:175], v[212:215], v[116:119], v[172:175]\n\t"
        "s_waitcnt lgkmcnt(2)\n\t"
        "v_add_f32 v226, v226, v149\n\t"
        "v_cvt_pk_bf16_f32 v198, v148, v149\n\t"
        "v_mfma_f32_16x16x32_bf16 v[160:163], v[216:219], v[24:27], v[160:163]\n\t"
        "ds_read_b128 v[212:215], v232 offset:41472\n\t"
        "v_add_f32 v226, v226, v150\n\t"
        "v_mfma_f32_16x16x32_bf16 v[164:167], v[216:219], v[56:59], v[164:167]\n\t"
        "v_exp_f32 v151, v151\n\t"
        "v_mfma_f32_16x16x32_bf16 v[168:171], v[216:219], v[88:91], v[168:171]\n\t"
        "v_exp_f32 v152, v152\n\t"
        "v_mfma_f32_16x16x32_bf16 v[172:175], v[216:219], v[120:123], v[172:175]\n\t"
        "s_waitcnt lgkmcnt(2)\n\t"
        "v_add_f32 v226, v226, v151\n\t"
        "v_add_f32 v228, v228, v152\n\t"
        "v_mfma_f32_16x16x32_bf16 v[160:163], v[220:223], v[28:31], v[160:163]\n\t"
        "ds_read_b128 v[216:219], v232 offset:41984\n\t"
        "v_cvt_pk_bf16_f32 v199, v150, v151\n\t"
        "v_mfma_f32_16x16x32_bf16 v[164:167], v[220:223], v[60:63], v[164:167]\n\t"
        "v_exp_f32 v153, v153\n\t"
        "v_mfma_f32_16x16x32_bf16 v[168:171], v[220:223], v[92:95], v[168:171]\n\t"
        "v_exp_f32 v154, v154\n\t"
        "v_mfma_f32_16x16x32_bf16 v[172:175], v[220:223], v[124:127], v[172:175]\n\t"
        "s_waitcnt lgkmcnt(2)\n\t"
        "v_add_f32 v228, v228, v153\n\t"
        "v_fma_f32 v157, v157, s56, v231\n\t"
        "v_mfma_f32_16x16x32_bf16 v[176:179], v[208:211], v[0:3], v[244:247]\n\t"
        "ds_read_b128 v[220:223], v232 offset:42496\n\t"
        "v_add_f32 v228, v228, v154\n\t"
        "v_mfma_f32_16x16x32_bf16 v[180:183], v[208:211], v[32:35], v[244:247]\n\t"
        "v_exp_f32 v155, v155\n\t"
        "v_mfma_f32_16x16x32_bf16 v[184:187], v[208:211], v[64:67], v[244:247]\n\t"
        "v_add_f32 v228, v228, v155\n\t"
        "v_cvt_pk_bf16_f32 v202, v152, v153\n\t"
        "v_mfma_f32_16x16x32_bf16 v[188:191], v[208:211], v[96:99], v[244:247]\n\t"
        "s_waitcnt lgkmcnt(2)\n\t"
        "v_fma_f32 v158, v158, s56, v231\n\t"
        "v_cvt_pk_bf16_f32 v203, v154, v155\n\t"
        "v_mfma_f32_16x16x32_bf16 v[176:179], v[212:215], v[4:7], v[176:179]\n\t"
        "ds_read_b128 v[208:211], v232 offset:43008\n\t"
        "v_fma_f32 v159, v159, s56, v231\n\t"
        "v_mfma_f32_16x16x32_bf16 v[180:183], v[212:215], v[36:39], v[180:183]\n\t"
        "v_exp_f32 v156, v156\n\t"
        "v_mfma_f32_16x16x32_bf16 v[184:187], v[212:215], v[68:71], v[184:187]\n\t"
        "v_add_f32 v230, v230, v156\n\t"
        "v_mfma_f32_16x16x32_bf16 v[188:191], v[212:215], v[100:103], v[188:191]\n\t"
        "s_waitcnt lgkmcnt(2)\n\t"
        "v_mfma_f32_16x16x32_bf16 v[176:179], v[216:219], v[8:11], v[176:179]\n\t"
        "ds_read_b128 v[212:215], v232 offset:43520\n\t"
        "v_mfma_f32_16x16x32_bf16 v[180:183], v[216:219], v[40:43], v[180:183]\n\t"
        "v_exp_f32 v157, v157\n\t"
        "v_mfma_f32_16x16x32_bf16 v[184:187], v[216:219], v[72:75], v[184:187]\n\t"
        "v_add_f32 v230, v230, v157\n\t"
        "v_cvt_pk_bf16_f32 v206, v156, v157\n\t"
        "v_mfma_f32_16x16x32_bf16 v[188:191], v[216:219], v[104:107], v[188:191]\n\t"
        "s_waitcnt lgkmcnt(2)\n\t"
        "v_mfma_f32_16x16x32_bf16 v[176:179], v[220:223], v[12:15], v[176:179]\n\t"
        "ds_read_b128 v[216:219], v232 offset:44032\n\t"
        "v_mfma_f32_16x16x32_bf16 v[180:183], v[220:223], v[44:47], v[180:183]\n\t"
        "v_exp_f32 v158, v158\n\t"
        "v_mfma_f32_16x16x32_bf16 v[184:187], v[220:223], v[76:79], v[184:187]\n\t"
        "v_readlane_b32 s61, v237, s58\n\t"
        "v_add_f32 v230, v230, v158\n\t"
        "v_mfma_f32_16x16x32_bf16 v[188:191], v[220:223], v[108:111], v[188:191]\n\t"
        "s_waitcnt lgkmcnt(2)\n\t"
        "v_mfma_f32_16x16x32_bf16 v[176:179], v[208:211], v[16:19], v[176:179]\n\t"
        "s_waitcnt vmcnt(9)\n\t"
        "ds_read_b128 v[220:223], v232 offset:44544\n\t"
        "v_mfma_f32_16x16x32_bf16 v[180:183], v[208:211], v[48:51], v[180:183]\n\t"
        "v_exp_f32 v159, v159\n\t"
        "v_mfma_f32_16x16x32_bf16 v[184:187], v[208:211], v[80:83], v[184:187]\n\t"
        "s_barrier\n\t"
        "v_cvt_pk_bf16_f32 v207, v158, v159\n\t"
        "v_add_f32 v230, v230, v159\n\t"
        "v_mfma_f32_16x16x32_bf16 v[188:191], v[208:211], v[112:115], v[188:191]\n\t"
        "s_waitcnt lgkmcnt(2)\n\t"
        "v_mfma_f32_16x16x32_bf16 v[176:179], v[212:215], v[20:23], v[176:179]\n\t"
        "ds_read_b64_tr_b16 v[208:209], v233 offset:0\n\t"
        "ds_read_b64_tr_b16 v[210:211], v233 offset:8192\n\t"
        "v_mfma_f32_16x16x32_bf16 v[180:183], v[212:215], v[52:55], v[180:183]\n\t"
        "s_add_u32 m0, s62, 0x18000\n\t"
        "v_mfma_f32_16x16x32_bf16 v[184:187], v[212:215], v[84:87], v[184:187]\n\t"
        "buffer_load_dwordx4 v235, s[44:47], s61 offen offset:0 lds\n\t"
        "v_mfma_f32_16x16x32_bf16 v[188:191], v[212:215], v[116:119], v[188:191]\n\t"
        "s_waitcnt lgkmcnt(3)\n\t"
        "v_mfma_f32_16x16x32_bf16 v[176:179], v[216:219], v[24:27], v[176:179]\n\t"
        "s_add_u32 s58, s58, 1\n\t"
        "ds_read_b64_tr_b16 v[212:213], v234 offset:0\n\t"
        "ds_read_b64_tr_b16 v[214:215], v234 offset:8192\n\t"
        "v_mfma_f32_16x16x32_bf16 v[180:183], v[216:219], v[56:59], v[180:183]\n\t"
        "s_add_u32 m0, s62, 0x18380\n\t"
        "v_max3_f32 v250, v224, v226, v228\n\t"
        "v_max_f32 v250, v250, v230\n\t"
        "v_mfma_f32_16x16x32_bf16 v[184:187], v[216:219], v[88:91], v[184:187]\n\t"
        "buffer_load_dwordx4 v235, s[44:47], s61 offen offset:128 lds\n\t"
        "v_mfma_f32_16x16x32_bf16 v[188:191], v[216:219], v[120:123], v[188:191]\n\t"
        "s_waitcnt lgkmcnt(4)\n\t"
        "v_cmp_lt_f32 vcc, s63, v250\n\t"
        "v_mfma_f32_16x16x32_bf16 v[176:179], v[220:223], v[28:31], v[176:179]\n\t"
        "s_cbranch_vccnz RSTUB1_%=\n\t"
        "RRET1_%=:\n\t"
        "ds_read_b64_tr_b16 v[216:217], v233 offset:512\n\t"
        "ds_read_b64_tr_b16 v[218:219], v233 offset:8704\n\t"
        "v_mfma_f32_16x16x32_bf16 v[180:183], v[220:223], v[60:63], v[180:183]\n\t"
        "s_add_u32 m0, s62, 0x18700\n\t"
        "v_fma_f32 v160, v160, s56, v225\n\t"
        "v_fma_f32 v161, v161, s56, v225\n\t"
        "v_mfma_f32_16x16x32_bf16 v[184:187], v[220:223], v[92:95], v[184:187]\n\t"
        "buffer_load_dwordx4 v235, s[44:47], s61 offen offset:256 lds\n\t"
        "v_mfma_f32_16x16x32_bf16 v[188:191], v[220:223], v[124:127], v[188:191]\n\t"
        "s_waitcnt lgkmcnt(4)\n\t"
        "v_fma_f32 v162, v162, s56, v225\n\t"
        "v_fma_f32 v163, v163, s56, v225\n\t"
        "v_mfma_f32_16x16x32_bf16 a[0:3], v[208:211], v[192:195], a[0:3]\n\t"
        "ds_read_b64_tr_b16 v[220:221], v234 offset:512\n\t"
        "ds_read_b64_tr_b16 v[222:223], v234 offset:8704\n\t"
        "v_mfma_f32_16x16x32_bf16 a[4:7], v[208:211], v[196:199], a[4:7]\n\t"
        "s_add_u32 m0, s62, 0x18a80\n\t"
        "v_exp_f32 v160, v160\n\t"
        "v_mfma_f32_16x16x32_bf16 a[8:11], v[208:211], v[200:203], a[8:11]\n\t"
        "buffer_load_dwordx4 v235, s[44:47], s61 offen offset:384 lds\n\t"
        "v_mfma_f32_16x16x32_bf16 a[12:15], v[208:211], v[204:207], a[12:15]\n\t"
        "s_waitcnt lgkmcnt(4)\n\t"
        "v_add_f32 v224, v224, v160\n\t"
        "v_fma_f32 v164, v164, s56, v227\n\t"
        "v_mfma_f32_16x16x32_bf16 a[16:19], v[212:215], v[192:195], a[16:19]\n\t"
        "ds_read_b64_tr_b16 v[208:209], v233 offset:1024\n\t"
        "ds_read_b64_tr_b16 v[210:211], v233 offset:9216\n\t"
        "v_mfma_f32_16x16x32_bf16 a[20:23], v[212:215], v[196:199], a[20:23]\n\t"
        "s_add_u32 m0, s62, 0x19000\n\t"
        "v_exp_f32 v161, v161\n\t"
        "v_mfma_f32_16x16x32_bf16 a[24:27], v[212:215], v[200:203], a[24:27]\n\t"
        "buffer_load_dwordx4 v236, s[44:47], s61 offen offset:0 lds\n\t"
        "v_mfma_f32_16x16x32_bf16 a[28:31], v[212:215], v[204:207], a[28:31]\n\t"
        "s_waitcnt lgkmcnt(4)\n\t"
        "v_add_f32 v224, v224, v161\n\t"
        "v_fma_f32 v165, v165, s56, v227\n\t"
        "v_mfma_f32_16x16x32_bf16 a[32:35], v[216:219], v[192:195], a[32:35]\n\t"
        "ds_read_b64_tr_b16 v[212:213], v234 offset:1024\n\t"
        "ds_read_b64_tr_b16 v[214:215], v234 offset:9216\n\t"
        "v_mfma_f32_16x16x32_bf16 a[36:39], v[216:219], v[196:199], a[36:39]\n\t"
        "s_add_u32 m0, s62, 0x19380\n\t"
        "v_exp_f32 v162, v162\n\t"
        "v_mfma_f32_16x16x32_bf16 a[40:43], v[216:219], v[200:203], a[40:43]\n\t"
        "buffer_load_dwordx4 v236, s[44:47], s61 offen offset:128 lds\n\t"
        "v_mfma_f32_16x16x32_bf16 a[44:47], v[216:219], v[204:207], a[44:47]\n\t"
        "s_waitcnt lgkmcnt(4)\n\t"
        "v_readlane_b32 s81, v237, s80\n\t"
        "v_add_f32 v224, v224, v162\n\t"
        "v_mfma_f32_16x16x32_bf16 a[48:51], v[220:223], v[192:195], a[48:51]\n\t"
        "ds_read_b64_tr_b16 v[216:217], v233 offset:1536\n\t"
        "ds_read_b64_tr_b16 v[218:219], v233 offset:9728\n\t"
        "v_mfma_f32_16x16x32_bf16 a[52:55], v[220:223], v[196:199], a[52:55]\n\t"
        "s_add_u32 m0, s62, 0x19700\n\t"
        "v_exp_f32 v163, v163\n\t"
        "v_mfma_f32_16x16x32_bf16 a[56:59], v[220:223], v[200:203], a[56:59]\n\t"
        "buffer_load_dwordx4 v236, s[44:47], s61 offen offset:256 lds\n\t"
        "v_mfma_f32_16x16x32_bf16 a[60:63], v[220:223], v[204:207], a[60:63]\n\t"
        "s_waitcnt lgkmcnt(4)\n\t"
        "v_add_f32 v224, v224, v163\n\t"
        "v_fma_f32 v166, v166, s56, v227\n\t"
        "v_mfma_f32_16x16x32_bf16 a[64:67], v[208:211], v[192:195], a[64:67]\n\t"
        "ds_read_b64_tr_b16 v[220:221], v234 offset:1536\n\t"
        "ds_read_b64_tr_b16 v[222:223], v234 offset:9728\n\t"
        "v_mfma_f32_16x16x32_bf16 a[68:71], v[208:211], v[196:199], a[68:71]\n\t"
        "s_add_u32 m0, s62, 0x19a80\n\t"
        "v_exp_f32 v164, v164\n\t"
        "v_mfma_f32_16x16x32_bf16 a[72:75], v[208:211], v[200:203], a[72:75]\n\t"
        "buffer_load_dwordx4 v236, s[44:47], s61 offen offset:384 lds\n\t"
        "v_mfma_f32_16x16x32_bf16 a[76:79], v[208:211], v[204:207], a[76:79]\n\t"
        "s_waitcnt lgkmcnt(4)\n\t"
        "v_add_f32 v226, v226, v164\n\t"
        "v_fma_f32 v167, v167, s56, v227\n\t"
        "v_mfma_f32_16x16x32_bf16 a[80:83], v[212:215], v[192:195], a[80:83]\n\t"
        "ds_read_b64_tr_b16 v[208:209], v251 offset:2048\n\t"
        "ds_read_b64_tr_b16 v[210:211], v251 offset:10240\n\t"
        "v_mfma_f32_16x16x32_bf16 a[84:87], v[212:215], v[196:199], a[84:87]\n\t"
        "v_exp_f32 v165, v165\n\t"
        "v_mfma_f32_16x16x32_bf16 a[88:91], v[212:215], v[200:203], a[88:91]\n\t"
        "buffer_load_dword v249, v248, s[76:79], s81 offen\n\t"
        "v_mfma_f32_16x16x32_bf16 a[92:95], v[212:215], v[204:207], a[92:95]\n\t"
        "s_waitcnt lgkmcnt(4)\n\t"
        "v_add_f32 v226, v226, v165\n\t"
        "v_fma_f32 v168, v168, s56, v229\n\t"
        "v_mfma_f32_16x16x32_bf16 a[96:99], v[216:219], v[192:195], a[96:99]\n\t"
        "ds_read_b64_tr_b16 v[212:213], v252 offset:2048\n\t"
        "ds_read_b64_tr_b16 v[214:215], v252 offset:10240\n\t"
        "v_mfma_f32_16x16x32_bf16 a[100:103], v[216:219], v[196:199], a[100:103]\n\t"
        "v_exp_f32 v166, v166\n\t"
        "v_mfma_f32_16x16x32_bf16 a[104:107], v[216:219], v[200:203], a[104:107]\n\t"
        "v_add_f32 v226, v226, v166\n\t"
        "v_fma_f32 v169, v169, s56, v229\n\t"
        "v_mfma_f32_16x16x32_bf16 a[108:111], v[216:219], v[204:207], a[108:111]\n\t"
        "s_waitcnt lgkmcnt(4)\n\t"
        "v_fma_f32 v170, v170, s56, v229\n\t"
        "v_fma_f32 v171, v171, s56, v229\n\t"
        "v_mfma_f32_16x16x32_bf16 a[112:115], v[220:223], v[192:195], a[112:115]\n\t"
        "ds_read_b64_tr_b16 v[216:217], v251 offset:2560\n\t"
        "ds_read_b64_tr_b16 v[218:219], v251 offset:10752\n\t"
        "v_mfma_f32_16x16x32_bf16 a[116:119], v[220:223], v[196:199], a[116:119]\n\t"
        "v_exp_f32 v167, v167\n\t"
        "v_mfma_f32_16x16x32_bf16 a[120:123], v[220:223], v[200:203], a[120:123]\n\t"
        "v_add_f32 v226, v226, v167\n\t"
        "v_fma_f32 v172, v172, s56, v231\n\t"
        "v_mfma_f32_16x16x32_bf16 a[124:127], v[220:223], v[204:207], a[124:127]\n\t"
        "s_waitcnt lgkmcnt(4)\n\t"
        "v_fma_f32 v173, v173, s56, v231\n\t"
        "v_fma_f32 v174, v174, s56, v231\n\t"
        "v_mfma_f32_16x16x32_bf16 a[128:131], v[208:211], v[192:195], a[128:131]\n\t"
        "ds_read_b64_tr_b16 v[220:221], v252 offset:2560\n\t"
        "ds_read_b64_tr_b16 v[222:223], v252 offset:10752\n\t"
        "v_mfma_f32_16x16x32_bf16 a[132:135], v[208:211], v[196:199], a[132:135]\n\t"
        "v_exp_f32 v168, v168\n\t"
        "v_mfma_f32_16x16x32_bf16 a[136:139], v[208:211], v[200:203], a[136:139]\n\t"
        "v_add_f32 v228, v228, v168\n\t"
        "v_fma_f32 v175, v175, s56, v231\n\t"
        "v_mfma_f32_16x16x32_bf16 a[140:143], v[208:211], v[204:207], a[140:143]\n\t"
        "s_waitcnt lgkmcnt(4)\n\t"
        "v_fma_f32 v176, v176, s56, v225\n\t"
        "v_fma_f32 v177, v177, s56, v225\n\t"
        "v_mfma_f32_16x16x32_bf16 a[144:147], v[212:215], v[192:195], a[144:147]\n\t"
        "ds_read_b64_tr_b16 v[208:209], v251 offset:3072\n\t"
        "ds_read_b64_tr_b16 v[210:211], v251 offset:11264\n\t"
        "v_mfma_f32_16x16x32_bf16 a[148:151], v[212:215], v[196:199], a[148:151]\n\t"
        "v_exp_f32 v169, v169\n\t"
        "v_mfma_f32_16x16x32_bf16 a[152:155], v[212:215], v[200:203], a[152:155]\n\t"
        "v_add_f32 v228, v228, v169\n\t"
        "v_fma_f32 v178, v178, s56, v225\n\t"
        "v_mfma_f32_16x16x32_bf16 a[156:159], v[212:215], v[204:207], a[156:159]\n\t"
        "s_waitcnt lgkmcnt(4)\n\t"
        "v_mfma_f32_16x16x32_bf16 a[160:163], v[216:219], v[192:195], a[160:163]\n\t"
        "ds_read_b64_tr_b16 v[212:213], v252 offset:3072\n\t"
        "ds_read_b64_tr_b16 v[214:215], v252 offset:11264\n\t"
        "v_mfma_f32_16x16x32_bf16 a[164:167], v[216:219], v[196:199], a[164:167]\n\t"
        "v_exp_f32 v170, v170\n\t"
        "v_mfma_f32_16x16x32_bf16 a[168:171], v[216:219], v[200:203], a[168:171]\n\t"
        "v_add_f32 v228, v228, v170\n\t"
        "v_fma_f32 v179, v179, s56, v225\n\t"
        "v_mfma_f32_16x16x32_bf16 a[172:175], v[216:219], v[204:207], a[172:175]\n\t"
        "s_waitcnt lgkmcnt(4)\n\t"
        "v_mfma_f32_16x16x32_bf16 a[176:179], v[220:223], v[192:195], a[176:179]\n\t"
        "ds_read_b64_tr_b16 v[216:217], v251 offset:3584\n\t"
        "ds_read_b64_tr_b16 v[218:219], v251 offset:11776\n\t"
        "v_mfma_f32_16x16x32_bf16 a[180:183], v[220:223], v[196:199], a[180:183]\n\t"
        "v_exp_f32 v171, v171\n\t"
        "v_mfma_f32_16x16x32_bf16 a[184:187], v[220:223], v[200:203], a[184:187]\n\t"
        "v_readlane_b32 s60, v237, s64\n\t"
        "v_add_f32 v228, v228, v171\n\t"
        "v_mfma_f32_16x16x32_bf16 a[188:191], v[220:223], v[204:207], a[188:191]\n\t"
        "s_waitcnt lgkmcnt(4)\n\t"
        "v_fma_f32 v180, v180, s56, v227\n\t"
        "v_mfma_f32_16x16x32_bf16 a[192:195], v[208:211], v[192:195], a[192:195]\n\t"
        "s_waitcnt vmcnt(9)\n\t"
        "ds_read_b64_tr_b16 v[220:221], v252 offset:3584\n\t"
        "ds_read_b64_tr_b16 v[222:223], v252 offset:11776\n\t"
        "v_mfma_f32_16x16x32_bf16 a[196:199], v[208:211], v[196:199], a[196:199]\n\t"
        "v_exp_f32 v172, v172\n\t"
        "v_mfma_f32_16x16x32_bf16 a[200:203], v[208:211], v[200:203], a[200:203]\n\t"
        "s_barrier\n\t"
        "v_add_f32 v230, v230, v172\n\t"
        "v_fma_f32 v181, v181, s56, v227\n\t"
        "v_mfma_f32_16x16x32_bf16 a[204:207], v[208:211], v[204:207], a[204:207]\n\t"
        "s_waitcnt lgkmcnt(4)\n\t"
        "v_fma_f32 v182, v182, s56, v227\n\t"
        "v_mfma_f32_16x16x32_bf16 a[208:211], v[212:215], v[192:195], a[208:211]\n\t"
        "ds_read_b128 v[208:211], v232 offset:0\n\t"
        "v_fma_f32 v183, v183, s56, v227\n\t"
        "v_mfma_f32_16x16x32_bf16 a[212:215], v[212:215], v[196:199], a[212:215]\n\t"
        "s_add_u32 m0, s62, 0x8000\n\t"
        "v_exp_f32 v173, v173\n\t"
        "v_mfma_f32_16x16x32_bf16 a[216:219], v[212:215], v[200:203], a[216:219]\n\t"
        "buffer_load_dwordx4 v235, s[40:43], s60 offen offset:0 lds\n\t"
        "v_mfma_f32_16x16x32_bf16 a[220:223], v[212:215], v[204:207], a[220:223]\n\t"
        "s_waitcnt lgkmcnt(3)\n\t"
        "v_add_f32 v230, v230, v173\n\t"
        "v_fma_f32 v184, v184, s56, v229\n\t"
        "v_mfma_f32_16x16x32_bf16 a[224:227], v[216:219], v[192:195], a[224:227]\n\t"
        "s_add_u32 s64, s64, 1\n\t"
        "ds_read_b128 v[212:215], v232 offset:512\n\t"
        "v_fma_f32 v185, v185, s56, v229\n\t"
        "v_mfma_f32_16x16x32_bf16 a[228:231], v[216:219], v[196:199], a[228:231]\n\t"
        "s_add_u32 m0, s62, 0x8380\n\t"
        "v_exp_f32 v174, v174\n\t"
        "v_mfma_f32_16x16x32_bf16 a[232:235], v[216:219], v[200:203], a[232:235]\n\t"
        "buffer_load_dwordx4 v235, s[40:43], s60 offen offset:128 lds\n\t"
        "v_mfma_f32_16x16x32_bf16 a[236:239], v[216:219], v[204:207], a[236:239]\n\t"
        "s_waitcnt lgkmcnt(2)\n\t"
        "v_add_f32 v230, v230, v174\n\t"
        "v_fma_f32 v186, v186, s56, v229\n\t"
        "v_mfma_f32_16x16x32_bf16 a[240:243], v[220:223], v[192:195], a[240:243]\n\t"
        "s_add_u32 s80, s80, 1\n\t"
        "ds_read_b128 v[216:219], v232 offset:1024\n\t"
        "v_fma_f32 v187, v187, s56, v229\n\t"
        "v_mfma_f32_16x16x32_bf16 a[244:247], v[220:223], v[196:199], a[244:247]\n\t"
        "s_add_u32 m0, s62, 0x8700\n\t"
        "v_exp_f32 v175, v175\n\t"
        "v_mfma_f32_16x16x32_bf16 a[248:251], v[220:223], v[200:203], a[248:251]\n\t"
        "buffer_load_dwordx4 v235, s[40:43], s60 offen offset:256 lds\n\t"
        "v_mfma_f32_16x16x32_bf16 a[252:255], v[220:223], v[204:207], a[252:255]\n\t"
        "v_add_f32 v230, v230, v175\n\t"
        "s_cmp_ge_u32 s58, s65\n\t"
        "s_cbranch_scc1 SPEC1_%=\n\t"
        "SRET1_%=:\n\t"
        "s_waitcnt lgkmcnt(2)\n\t"
        "v_mfma_f32_16x16x32_bf16 v[128:131], v[208:211], v[0:3], v[240:243]\n\t"
        "ds_read_b128 v[220:223], v232 offset:1536\n\t"
        "v_cvt_pk_bf16_f32 v192, v160, v161\n\t"
        "v_mfma_f32_16x16x32_bf16 v[132:135], v[208:211], v[32:35], v[240:243]\n\t"
        "s_add_u32 m0, s62, 0x8a80\n\t"
        "v_exp_f32 v176, v176\n\t"
        "v_mfma_f32_16x16x32_bf16 v[136:139], v[208:211], v[64:67], v[240:243]\n\t"
        "buffer_load_dwordx4 v235, s[40:43], s60 offen offset:384 lds\n\t"
        "v_mfma_f32_16x16x32_bf16 v[140:143], v[208:211], v[96:99], v[240:243]\n\t"
        "s_waitcnt lgkmcnt(2)\n\t"
        "v_add_f32 v224, v224, v176\n\t"
        "v_cvt_pk_bf16_f32 v193, v162, v163\n\t"
        "v_mfma_f32_16x16x32_bf16 v[128:131], v[212:215], v[4:7], v[128:131]\n\t"
        "ds_read_b128 v[208:211], v232 offset:2048\n\t"
        "v_cvt_pk_bf16_f32 v196, v164, v165\n\t"
        "v_mfma_f32_16x16x32_bf16 v[132:135], v[212:215], v[36:39], v[132:135]\n\t"
        "s_add_u32 m0, s62, 0x9000\n\t"
        "v_exp_f32 v177, v177\n\t"
        "v_mfma_f32_16x16x32_bf16 v[136:139], v[212:215], v[68:71], v[136:139]\n\t"
        "buffer_load_dwordx4 v236, s[40:43], s60 offen offset:0 lds\n\t"
        "v_mfma_f32_16x16x32_bf16 v[140:143], v[212:215], v[100:103], v[140:143]\n\t"
        "s_waitcnt lgkmcnt(2)\n\t"
        "v_cvt_pk_bf16_f32 v197, v166, v167\n\t"
        "v_cvt_pk_bf16_f32 v200, v168, v169\n\t"
        "v_mfma_f32_16x16x32_bf16 v[128:131], v[216:219], v[8:11], v[128:131]\n\t"
        "ds_read_b128 v[212:215], v232 offset:2560\n\t"
        "v_cvt_pk_bf16_f32 v201, v170, v171\n\t"
        "v_mfma_f32_16x16x32_bf16 v[132:135], v[216:219], v[40:43], v[132:135]\n\t"
        "s_add_u32 m0, s62, 0x9380\n\t"
        "v_exp_f32 v178, v178\n\t"
        "v_mfma_f32_16x16x32_bf16 v[136:139], v[216:219], v[72:75], v[136:139]\n\t"
        "buffer_load_dwordx4 v236, s[40:43], s60 offen offset:128 lds\n\t"
        "v_mfma_f32_16x16x32_bf16 v[140:143], v[216:219], v[104:107], v[140:143]\n\t"
        "s_waitcnt lgkmcnt(2)\n\t"
        "v_cvt_pk_bf16_f32 v204, v172, v173\n\t"
        "v_cvt_pk_bf16_f32 v205, v174, v175\n\t"
        "v_mfma_f32_16x16x32_bf16 v[128:131], v[220:223], v[12:15], v[128:131]\n\t"
        "ds_read_b128 v[216:219], v232 offset:3072\n\t"
        "v_add_f32 v224, v224, v177\n\t"
        "v_mfma_f32_16x16x32_bf16 v[132:135], v[220:223], v[44:47], v[132:135]\n\t"
        "s_add_u32 m0, s62, 0x9700\n\t"
        "v_exp_f32 v179, v179\n\t"
        "v_mfma_f32_16x16x32_bf16 v[136:139], v[220:223], v[76:79], v[136:139]\n\t"
        "buffer_load_dwordx4 v236, s[40:43], s60 offen offset:256 lds\n\t"
        "v_mfma_f32_16x16x32_bf16 v[140:143], v[220:223], v[108:111], v[140:143]\n\t"
        "s_waitcnt lgkmcnt(2)\n\t"
        "v_add_f32 v224, v224, v178\n\t"
        "v_cvt_pk_bf16_f32 v194, v176, v177\n\t"
        "v_mfma_f32_16x16x32_bf16 v[128:131], v[208:211], v[16:19], v[128:131]\n\t"
        "ds_read_b128 v[220:223], v232 offset:3584\n\t"
        "v_add_f32 v224, v224, v179\n\t"
        "v_mfma_f32_16x16x32_bf16 v[132:135], v[208:211], v[48:51], v[132:135]\n\t"
        "s_add_u32 m0, s62, 0x9a80\n\t"
        "v_exp_f32 v180, v180\n\t"
        "v_mfma_f32_16x16x32_bf16 v[136:139], v[208:211], v[80:83], v[136:139]\n\t"
        "buffer_load_dwordx4 v236, s[40:43], s60 offen offset:384 lds\n\t"
        "v_mfma_f32_16x16x32_bf16 v[140:143], v[208:211], v[112:115], v[140:143]\n\t"
        "s_waitcnt lgkmcnt(2)\n\t"
        "v_add_f32 v226, v226, v180\n\t"
        "v_cvt_pk_bf16_f32 v195, v178, v179\n\t"
        "v_mfma_f32_16x16x32_bf16 v[128:131], v[212:215], v[20:23], v[128:131]\n\t"
        "ds_read_b128 v[208:211], v232 offset:8192\n\t"
        "v_fma_f32 v188, v188, s56, v231\n\t"
        "v_mfma_f32_16x16x32_bf16 v[132:135], v[212:215], v[52:55], v[132:135]\n\t"
        "v_exp_f32 v181, v181\n\t"
        "v_mfma_f32_16x16x32_bf16 v[136:139], v[212:215], v[84:87], v[136:139]\n\t"
        "v_exp_f32 v182, v182\n\t"
        "v_mfma_f32_16x16x32_bf16 v[140:143], v[212:215], v[116:119], v[140:143]\n\t"
        "s_waitcnt lgkmcnt(2)\n\t"
        "v_add_f32 v226, v226, v181\n\t"
        "v_cvt_pk_bf16_f32 v198, v180, v181\n\t"
        "v_mfma_f32_16x16x32_bf16 v[128:131], v[216:219], v[24:27], v[128:131]\n\t"
        "ds_read_b128 v[212:215], v232 offset:8704\n\t"
        "v_add_f32 v226, v226, v182\n\t"
        "v_mfma_f32_16x16x32_bf16 v[132:135], v[216:219], v[56:59], v[132:135]\n\t"
        "v_exp_f32 v183, v183\n\t"
        "v_mfma_f32_16x16x32_bf16 v[136:139], v[216:219], v[88:91], v[136:139]\n\t"
        "v_exp_f32 v184, v184\n\t"
        "v_mfma_f32_16x16x32_bf16 v[140:143], v[216:219], v[120:123], v[140:143]\n\t"
        "s_waitcnt lgkmcnt(2)\n\t"
        "v_add_f32 v226, v226, v183\n\t"
        "v_add_f32 v228, v228, v184\n\t"
        "v_mfma_f32_16x16x32_bf16 v[128:131], v[220:223], v[28:31], v[128:131]\n\t"
        "ds_read_b128 v[216:219], v232 offset:9216\n\t"
        "v_cvt_pk_bf16_f32 v199, v182, v183\n\t"
        "v_mfma_f32_16x16x32_bf16 v[132:135], v[220:223], v[60:63], v[132:135]\n\t"
        "v_exp_f32 v185, v185\n\t"
        "v_mfma_f32_16x16x32_bf16 v[136:139], v[220:223], v[92:95], v[136:139]\n\t"
        "v_exp_f32 v186, v186\n\t"
        "v_mfma_f32_16x16x32_bf16 v[140:143], v[220:223], v[124:127], v[140:143]\n\t"
        "s_waitcnt lgkmcnt(2)\n\t"
        "v_add_f32 v228, v228, v185\n\t"
        "v_fma_f32 v189, v189, s56, v231\n\t"
        "v_mfma_f32_16x16x32_bf16 v[144:147], v[208:211], v[0:3], v[244:247]\n\t"
        "ds_read_b128 v[220:223], v232 offset:9728\n\t"
        "v_add_f32 v228, v228, v186\n\t"
        "v_mfma_f32_16x16x32_bf16 v[148:151], v[208:211], v[32:35], v[244:247]\n\t"
        "v_exp_f32 v187, v187\n\t"
        "v_mfma_f32_16x16x32_bf16 v[152:155], v[208:211], v[64:67], v[244:247]\n\t"
        "v_add_f32 v228, v228, v187\n\t"
        "v_cvt_pk_bf16_f32 v202, v184, v185\n\t"
        "v_mfma_f32_16x16x32_bf16 v[156:159], v[208:211], v[96:99], v[244:247]\n\t"
        "s_waitcnt lgkmcnt(2)\n\t"
        "v_fma_f32 v190, v190, s56, v231\n\t"
        "v_cvt_pk_bf16_f32 v203, v186, v187\n\t"
        "v_mfma_f32_16x16x32_bf16 v[144:147], v[212:215], v[4:7], v[144:147]\n\t"
        "ds_read_b128 v[208:211], v232 offset:10240\n\t"
        "v_fma_f32 v191, v191, s56, v231\n\t"
        "v_mfma_f32_16x16x32_bf16 v[148:151], v[212:215], v[36:39], v[148:151]\n\t"
        "v_exp_f32 v188, v188\n\t"
        "v_mfma_f32_16x16x32_bf16 v[152:155], v[212:215], v[68:71], v[152:155]\n\t"
        "v_add_f32 v230, v230, v188\n\t"
        "v_mfma_f32_16x16x32_bf16 v[156:159], v[212:215], v[100:103], v[156:159]\n\t"
        "s_waitcnt lgkmcnt(2)\n\t"
        "v_mfma_f32_16x16x32_bf16 v[144:147], v[216:219], v[8:11], v[144:147]\n\t"
        "ds_read_b128 v[212:215], v232 offset:10752\n\t"
        "v_mfma_f32_16x16x32_bf16 v[148:151], v[216:219], v[40:43], v[148:151]\n\t"
        "v_exp_f32 v189, v189\n\t"
        "v_mfma_f32_16x16x32_bf16 v[152:155], v[216:219], v[72:75], v[152:155]\n\t"
        "v_add_f32 v230, v230, v189\n\t"
        "v_cvt_pk_bf16_f32 v206, v188, v189\n\t"
        "v_mfma_f32_16x16x32_bf16 v[156:159], v[216:219], v[104:107], v[156:159]\n\t"
        "s_waitcnt lgkmcnt(2)\n\t"
        "v_mfma_f32_16x16x32_bf16 v[144:147], v[220:223], v[12:15], v[144:147]\n\t"
        "ds_read_b128 v[216:219], v232 offset:11264\n\t"
        "v_mfma_f32_16x16x32_bf16 v[148:151], v[220:223], v[44:47], v[148:151]\n\t"
        "v_exp_f32 v190, v190\n\t"
        "v_mfma_f32_16x16x32_bf16 v[152:155], v[220:223], v[76:79], v[152:155]\n\t"
        "v_readlane_b32 s61, v237, s58\n\t"
        "v_add_f32 v230, v230, v190\n\t"
        "v_mfma_f32_16x16x32_bf16 v[156:159], v[220:223], v[108:111], v[156:159]\n\t"
        "s_waitcnt lgkmcnt(2)\n\t"
        "v_mfma_f32_16x16x32_bf16 v[144:147], v[208:211], v[16:19], v[144:147]\n\t"
        "s_waitcnt vmcnt(9)\n\t"
        "ds_read_b128 v[220:223], v232 offset:11776\n\t"
        "v_mfma_f32_16x16x32_bf16 v[148:151], v[208:211], v[48:51], v[148:151]\n\t"
        "v_exp_f32 v191, v191\n\t"
        "v_mfma_f32_16x16x32_bf16 v[152:155], v[208:211], v[80:83], v[152:155]\n\t"
        "s_barrier\n\t"
        "v_cvt_pk_bf16_f32 v207, v190, v191\n\t"
        "v_add_f32 v230, v230, v191\n\t"
        "v_mfma_f32_16x16x32_bf16 v[156:159], v[208:211], v[112:115], v[156:159]\n\t"
        "s_waitcnt lgkmcnt(2)\n\t"
        "v_mfma_f32_16x16x32_bf16 v[144:147], v[212:215], v[20:23], v[144:147]\n\t"
        "ds_read_b64_tr_b16 v[208:209], v233 offset:32768\n\t"
        "ds_read_b64_tr_b16 v[210:211], v233 offset:40960\n\t"
        "v_mfma_f32_16x16x32_bf16 v[148:151], v[212:215], v[52:55], v[148:151]\n\t"
        "s_add_u32 m0, s62, 0x10000\n\t"
        "v_mfma_f32_16x16x32_bf16 v[152:155], v[212:215], v[84:87], v[152:155]\n\t"
        "buffer_load_dwordx4 v235, s[44:47], s61 offen offset:0 lds\n\t"
        "v_mfma_f32_16x16x32_bf16 v[156:159], v[212:215], v[116:119], v[156:159]\n\t"
        "s_waitcnt lgkmcnt(3)\n\t"
        "v_mfma_f32_16x16x32_bf16 v[144:147], v[216:219], v[24:27], v[144:147]\n\t"
        "s_add_u32 s58, s58, 1\n\t"
        "ds_read_b64_tr_b16 v[212:213], v234 offset:32768\n\t"
        "ds_read_b64_tr_b16 v[214:215], v234 offset:40960\n\t"
        "v_mfma_f32_16x16x32_bf16 v[148:151], v[216:219], v[56:59], v[148:151]\n\t"
        "s_add_u32 m0, s62, 0x10380\n\t"
        "v_max3_f32 v250, v224, v226, v228\n\t"
        "v_max_f32 v250, v250, v230\n\t"
        "v_mfma_f32_16x16x32_bf16 v[152:155], v[216:219], v[88:91], v[152:155]\n\t"
        "buffer_load_dwordx4 v235, s[44:47], s61 offen offset:128 lds\n\t"
        "v_mfma_f32_16x16x32_bf16 v[156:159], v[216:219], v[120:123], v[156:159]\n\t"
        "s_waitcnt lgkmcnt(4)\n\t"
        "v_cmp_lt_f32 vcc, s63, v250\n\t"
        "v_mfma_f32_16x16x32_bf16 v[144:147], v[220:223], v[28:31], v[144:147]\n\t"
        "s_cbranch_vccnz RSTUB0_%=\n\t"
        "RRET0_%=:\n\t"
        "ds_read_b64_tr_b16 v[216:217], v233 offset:33280\n\t"
        "ds_read_b64_tr_b16 v[218:219], v233 offset:41472\n\t"
        "v_mfma_f32_16x16x32_bf16 v[148:151], v[220:223], v[60:63], v[148:151]\n\t"
        "s_add_u32 m0, s62, 0x10700\n\t"
        "v_fma_f32 v128, v128, s56, v225\n\t"
        "v_fma_f32 v129, v129, s56, v225\n\t"
        "v_mfma_f32_16x16x32_bf16 v[152:155], v[220:223], v[92:95], v[152:155]\n\t"
        "buffer_load_dwordx4 v235, s[44:47], s61 offen offset:256 lds\n\t"
        "v_mfma_f32_16x16x32_bf16 v[156:159], v[220:223], v[124:127], v[156:159]\n\t"
        "s_waitcnt lgkmcnt(4)\n\t"
        "v_fma_f32 v130, v130, s56, v225\n\t"
        "v_fma_f32 v131, v131, s56, v225\n\t"
        "v_mfma_f32_16x16x32_bf16 a[0:3], v[208:211], v[192:195], a[0:3]\n\t"
        "ds_read_b64_tr_b16 v[220:221], v234 offset:33280\n\t"
        "ds_read_b64_tr_b16 v[222:223], v234 offset:41472\n\t"
        "v_mfma_f32_16x16x32_bf16 a[4:7], v[208:211], v[196:199], a[4:7]\n\t"
        "s_add_u32 m0, s62, 0x10a80\n\t"
        "v_exp_f32 v128, v128\n\t"
        "v_mfma_f32_16x16x32_bf16 a[8:11], v[208:211], v[200:203], a[8:11]\n\t"
        "buffer_load_dwordx4 v235, s[44:47], s61 offen offset:384 lds\n\t"
        "v_mfma_f32_16x16x32_bf16 a[12:15], v[208:211], v[204:207], a[12:15]\n\t"
        "s_waitcnt lgkmcnt(4)\n\t"
        "v_add_f32 v224, v224, v128\n\t"
        "v_fma_f32 v132, v132, s56, v227\n\t"
        "v_mfma_f32_16x16x32_bf16 a[16:19], v[212:215], v[192:195], a[16:19]\n\t"
        "ds_read_b64_tr_b16 v[208:209], v233 offset:33792\n\t"
        "ds_read_b64_tr_b16 v[210:211], v233 offset:41984\n\t"
        "v_mfma_f32_16x16x32_bf16 a[20:23], v[212:215], v[196:199], a[20:23]\n\t"
        "s_add_u32 m0, s62, 0x11000\n\t"
        "v_exp_f32 v129, v129\n\t"
        "v_mfma_f32_16x16x32_bf16 a[24:27], v[212:215], v[200:203], a[24:27]\n\t"
        "buffer_load_dwordx4 v236, s[44:47], s61 offen offset:0 lds\n\t"
        "v_mfma_f32_16x16x32_bf16 a[28:31], v[212:215], v[204:207], a[28:31]\n\t"
        "s_waitcnt lgkmcnt(4)\n\t"
        "v_add_f32 v224, v224, v129\n\t"
        "v_fma_f32 v133, v133, s56, v227\n\t"
        "v_mfma_f32_16x16x32_bf16 a[32:35], v[216:219], v[192:195], a[32:35]\n\t"
        "ds_read_b64_tr_b16 v[212:213], v234 offset:33792\n\t"
        "ds_read_b64_tr_b16 v[214:215], v234 offset:41984\n\t"
        "v_mfma_f32_16x16x32_bf16 a[36:39], v[216:219], v[196:199], a[36:39]\n\t"
        "s_add_u32 m0, s62, 0x11380\n\t"
        "v_exp_f32 v130, v130\n\t"
        "v_mfma_f32_16x16x32_bf16 a[40:43], v[216:219], v[200:203], a[40:43]\n\t"
        "buffer_load_dwordx4 v236, s[44:47], s61 offen offset:128 lds\n\t"
        "v_mfma_f32_16x16x32_bf16 a[44:47], v[216:219], v[204:207], a[44:47]\n\t"
        "s_waitcnt lgkmcnt(4)\n\t"
        "v_readlane_b32 s81, v237, s80\n\t"
        "v_add_f32 v224, v224, v130\n\t"
        "v_mfma_f32_16x16x32_bf16 a[48:51], v[220:223], v[192:195], a[48:51]\n\t"
        "ds_read_b64_tr_b16 v[216:217], v233 offset:34304\n\t"
        "ds_read_b64_tr_b16 v[218:219], v233 offset:42496\n\t"
        "v_mfma_f32_16x16x32_bf16 a[52:55], v[220:223], v[196:199], a[52:55]\n\t"
        "s_add_u32 m0, s62, 0x11700\n\t"
        "v_exp_f32 v131, v131\n\t"
        "v_mfma_f32_16x16x32_bf16 a[56:59], v[220:223], v[200:203], a[56:59]\n\t"
        "buffer_load_dwordx4 v236, s[44:47], s61 offen offset:256 lds\n\t"
        "v_mfma_f32_16x16x32_bf16 a[60:63], v[220:223], v[204:207], a[60:63]\n\t"
        "s_waitcnt lgkmcnt(4)\n\t"
        "v_add_f32 v224, v224, v131\n\t"
        "v_fma_f32 v134, v134, s56, v227\n\t"
        "v_mfma_f32_16x16x32_bf16 a[64:67], v[208:211], v[192:195], a[64:67]\n\t"
        "ds_read_b64_tr_b16 v[220:221], v234 offset:34304\n\t"
        "ds_read_b64_tr_b16 v[222:223], v234 offset:42496\n\t"
        "v_mfma_f32_16x16x32_bf16 a[68:71], v[208:211], v[196:199], a[68:71]\n\t"
        "s_add_u32 m0, s62, 0x11a80\n\t"
        "v_exp_f32 v132, v132\n\t"
        "v_mfma_f32_16x16x32_bf16 a[72:75], v[208:211], v[200:203], a[72:75]\n\t"
        "buffer_load_dwordx4 v236, s[44:47], s61 offen offset:384 lds\n\t"
        "v_mfma_f32_16x16x32_bf16 a[76:79], v[208:211], v[204:207], a[76:79]\n\t"
        "s_waitcnt lgkmcnt(4)\n\t"
        "v_add_f32 v226, v226, v132\n\t"
        "v_fma_f32 v135, v135, s56, v227\n\t"
        "v_mfma_f32_16x16x32_bf16 a[80:83], v[212:215], v[192:195], a[80:83]\n\t"
        "ds_read_b64_tr_b16 v[208:209], v251 offset:34816\n\t"
        "ds_read_b64_tr_b16 v[210:211], v251 offset:43008\n\t"
        "v_mfma_f32_16x16x32_bf16 a[84:87], v[212:215], v[196:199], a[84:87]\n\t"
        "v_exp_f32 v133, v133\n\t"
        "v_mfma_f32_16x16x32_bf16 a[88:91], v[212:215], v[200:203], a[88:91]\n\t"
        "buffer_load_dword v249, v248, s[76:79], s81 offen\n\t"
        "v_mfma_f32_16x16x32_bf16 a[92:95], v[212:215], v[204:207], a[92:95]\n\t"
        "s_waitcnt lgkmcnt(4)\n\t"
        "v_add_f32 v226, v226, v133\n\t"
        "v_fma_f32 v136, v136, s56, v229\n\t"
        "v_mfma_f32_16x16x32_bf16 a[96:99], v[216:219], v[192:195], a[96:99]\n\t"
        "ds_read_b64_tr_b16 v[212:213], v252 offset:34816\n\t"
        "ds_read_b64_tr_b16 v[214:215], v252 offset:43008\n\t"
        "v_mfma_f32_16x16x32_bf16 a[100:103], v[216:219], v[196:199], a[100:103]\n\t"
        "v_exp_f32 v134, v134\n\t"
        "v_mfma_f32_16x16x32_bf16 a[104:107], v[216:219], v[200:203], a[104:107]\n\t"
        "v_add_f32 v226, v226, v134\n\t"
        "v_fma_f32 v137, v137, s56, v229\n\t"
        "v_mfma_f32_16x16x32_bf16 a[108:111], v[216:219], v[204:207], a[108:111]\n\t"
        "s_waitcnt lgkmcnt(4)\n\t"
        "v_fma_f32 v138, v138, s56, v229\n\t"
        "v_fma_f32 v139, v139, s56, v229\n\t"
        "v_mfma_f32_16x16x32_bf16 a[112:115], v[220:223], v[192:195], a[112:115]\n\t"
        "ds_read_b64_tr_b16 v[216:217], v251 offset:35328\n\t"
        "ds_read_b64_tr_b16 v[218:219], v251 offset:43520\n\t"
        "v_mfma_f32_16x16x32_bf16 a[116:119], v[220:223], v[196:199], a[116:119]\n\t"
        "v_exp_f32 v135, v135\n\t"
        "v_mfma_f32_16x16x32_bf16 a[120:123], v[220:223], v[200:203], a[120:123]\n\t"
        "v_add_f32 v226, v226, v135\n\t"
        "v_fma_f32 v140, v140, s56, v231\n\t"
        "v_mfma_f32_16x16x32_bf16 a[124:127], v[220:223], v[204:207], a[124:127]\n\t"
        "s_waitcnt lgkmcnt(4)\n\t"
        "v_fma_f32 v141, v141, s56, v231\n\t"
        "v_fma_f32 v142, v142, s56, v231\n\t"
        "v_mfma_f32_16x16x32_bf16 a[128:131], v[208:211], v[192:195], a[128:131]\n\t"
        "ds_read_b64_tr_b16 v[220:221], v252 offset:35328\n\t"
        "ds_read_b64_tr_b16 v[222:223], v252 offset:43520\n\t"
        "v_mfma_f32_16x16x32_bf16 a[132:135], v[208:211], v[196:199], a[132:135]\n\t"
        "v_exp_f32 v136, v136\n\t"
        "v_mfma_f32_16x16x32_bf16 a[136:139], v[208:211], v[200:203], a[136:139]\n\t"
        "v_add_f32 v228, v228, v136\n\t"
        "v_fma_f32 v143, v143, s56, v231\n\t"
        "v_mfma_f32_16x16x32_bf16 a[140:143], v[208:211], v[204:207], a[140:143]\n\t"
        "s_waitcnt lgkmcnt(4)\n\t"
        "v_fma_f32 v144, v144, s56, v225\n\t"
        "v_fma_f32 v145, v145, s56, v225\n\t"
        "v_mfma_f32_16x16x32_bf16 a[144:147], v[212:215], v[192:195], a[144:147]\n\t"
        "ds_read_b64_tr_b16 v[208:209], v251 offset:35840\n\t"
        "ds_read_b64_tr_b16 v[210:211], v251 offset:44032\n\t"
        "v_mfma_f32_16x16x32_bf16 a[148:151], v[212:215], v[196:199], a[148:151]\n\t"
        "v_exp_f32 v137, v137\n\t"
        "v_mfma_f32_16x16x32_bf16 a[152:155], v[212:215], v[200:203], a[152:155]\n\t"
        "v_add_f32 v228, v228, v137\n\t"
        "v_fma_f32 v146, v146, s56, v225\n\t"
        "v_mfma_f32_16x16x32_bf16 a[156:159], v[212:215], v[204:207], a[156:159]\n\t"
        "s_waitcnt lgkmcnt(4)\n\t"
        "v_mfma_f32_16x16x32_bf16 a[160:163], v[216:219], v[192:195], a[160:163]\n\t"
        "ds_read_b64_tr_b16 v[212:213], v252 offset:35840\n\t"
        "ds_read_b64_tr_b16 v[214:215], v252 offset:44032\n\t"
        "v_mfma_f32_16x16x32_bf16 a[164:167], v[216:219], v[196:199], a[164:167]\n\t"
        "v_exp_f32 v138, v138\n\t"
        "v_mfma_f32_16x16x32_bf16 a[168:171], v[216:219], v[200:203], a[168:171]\n\t"
        "v_add_f32 v228, v228, v138\n\t"
        "v_fma_f32 v147, v147, s56, v225\n\t"
        "v_mfma_f32_16x16x32_bf16 a[172:175], v[216:219], v[204:207], a[172:175]\n\t"
        "s_waitcnt lgkmcnt(4)\n\t"
        "v_mfma_f32_16x16x32_bf16 a[176:179], v[220:223], v[192:195], a[176:179]\n\t"
        "ds_read_b64_tr_b16 v[216:217], v251 offset:36352\n\t"
        "ds_read_b64_tr_b16 v[218:219], v251 offset:44544\n\t"
        "v_mfma_f32_16x16x32_bf16 a[180:183], v[220:223], v[196:199], a[180:183]\n\t"
        "v_exp_f32 v139, v139\n\t"
        "v_mfma_f32_16x16x32_bf16 a[184:187], v[220:223], v[200:203], a[184:187]\n\t"
        "v_readlane_b32 s60, v237, s64\n\t"
        "v_add_f32 v228, v228, v139\n\t"
        "v_mfma_f32_16x16x32_bf16 a[188:191], v[220:223], v[204:207], a[188:191]\n\t"
        "s_waitcnt lgkmcnt(4)\n\t"
        "v_fma_f32 v148, v148, s56, v227\n\t"
        "v_mfma_f32_16x16x32_bf16 a[192:195], v[208:211], v[192:195], a[192:195]\n\t"
        "s_waitcnt vmcnt(9)\n\t"
        "ds_read_b64_tr_b16 v[220:221], v252 offset:36352\n\t"
        "ds_read_b64_tr_b16 v[222:223], v252 offset:44544\n\t"
        "v_mfma_f32_16x16x32_bf16 a[196:199], v[208:211], v[196:199], a[196:199]\n\t"
        "v_exp_f32 v140, v140\n\t"
        "v_mfma_f32_16x16x32_bf16 a[200:203], v[208:211], v[200:203], a[200:203]\n\t"
        "s_barrier\n\t"
        "v_add_f32 v230, v230, v140\n\t"
        "v_fma_f32 v149, v149, s56, v227\n\t"
        "v_mfma_f32_16x16x32_bf16 a[204:207], v[208:211], v[204:207], a[204:207]\n\t"
        "s_waitcnt lgkmcnt(4)\n\t"
        "v_fma_f32 v150, v150, s56, v227\n\t"
        "v_mfma_f32_16x16x32_bf16 a[208:211], v[212:215], v[192:195], a[208:211]\n\t"
        "ds_read_b128 v[208:211], v232 offset:32768\n\t"
        "v_fma_f32 v151, v151, s56, v227\n\t"
        "v_mfma_f32_16x16x32_bf16 a[212:215], v[212:215], v[196:199], a[212:215]\n\t"
        "s_add_u32 m0, s62, 0x0\n\t"
        "v_exp_f32 v141, v141\n\t"
        "v_mfma_f32_16x16x32_bf16 a[216:219], v[212:215], v[200:203], a[216:219]\n\t"
        "buffer_load_dwordx4 v235, s[40:43], s60 offen offset:0 lds\n\t"
        "v_mfma_f32_16x16x32_bf16 a[220:223], v[212:215], v[204:207], a[220:223]\n\t"
        "s_waitcnt lgkmcnt(3)\n\t"
        "v_add_f32 v230, v230, v141\n\t"
        "v_fma_f32 v152, v152, s56, v229\n\t"
        "v_mfma_f32_16x16x32_bf16 a[224:227], v[216:219], v[192:195], a[224:227]\n\t"
        "s_add_u32 s64, s64, 1\n\t"
        "ds_read_b128 v[212:215], v232 offset:33280\n\t"
        "v_fma_f32 v153, v153, s56, v229\n\t"
        "v_mfma_f32_16x16x32_bf16 a[228:231], v[216:219], v[196:199], a[228:231]\n\t"
        "s_add_u32 m0, s62, 0x380\n\t"
        "v_exp_f32 v142, v142\n\t"
        "v_mfma_f32_16x16x32_bf16 a[232:235], v[216:219], v[200:203], a[232:235]\n\t"
        "buffer_load_dwordx4 v235, s[40:43], s60 offen offset:128 lds\n\t"
        "v_mfma_f32_16x16x32_bf16 a[236:239], v[216:219], v[204:207], a[236:239]\n\t"
        "s_waitcnt lgkmcnt(2)\n\t"
        "v_add_f32 v230, v230, v142\n\t"
        "v_fma_f32 v154, v154, s56, v229\n\t"
        "v_mfma_f32_16x16x32_bf16 a[240:243], v[220:223], v[192:195], a[240:243]\n\t"
        "s_add_u32 s80, s80, 1\n\t"
        "ds_read_b128 v[216:219], v232 offset:33792\n\t"
        "v_fma_f32 v155, v155, s56, v229\n\t"
        "v_mfma_f32_16x16x32_bf16 a[244:247], v[220:223], v[196:199], a[244:247]\n\t"
        "s_add_u32 m0, s62, 0x700\n\t"
        "v_exp_f32 v143, v143\n\t"
        "v_mfma_f32_16x16x32_bf16 a[248:251], v[220:223], v[200:203], a[248:251]\n\t"
        "buffer_load_dwordx4 v235, s[40:43], s60 offen offset:256 lds\n\t"
        "v_mfma_f32_16x16x32_bf16 a[252:255], v[220:223], v[204:207], a[252:255]\n\t"
        "v_add_f32 v230, v230, v143\n\t"
        "s_cmp_ge_u32 s58, s65\n\t"
        "s_cbranch_scc1 SPEC0_%=\n\t"
        "SRET0_%=:\n\t"
        "s_branch LOOP_%=\n\t"
        "TAIL0_%=:\n\t"
        "v_fma_f32 v156, v156, s56, v231\n\t"
        "v_fma_f32 v157, v157, s56, v231\n\t"
        "v_fma_f32 v158, v158, s56, v231\n\t"
        "v_fma_f32 v159, v159, s56, v231\n\t"
        "v_exp_f32 v144, v144\n\t"
        "v_exp_f32 v145, v145\n\t"
        "v_exp_f32 v146, v146\n\t"
        "v_exp_f32 v147, v147\n\t"
        "v_exp_f32 v148, v148\n\t"
        "v_exp_f32 v149, v149\n\t"
        "v_exp_f32 v150, v150\n\t"
        "v_exp_f32 v151, v151\n\t"
        "v_exp_f32 v152, v152\n\t"
        "v_exp_f32 v153, v153\n\t"
        "v_exp_f32 v154, v154\n\t"
        "v_exp_f32 v155, v155\n\t"
        "v_exp_f32 v156, v156\n\t"
        "v_exp_f32 v157, v157\n\t"
        "v_exp_f32 v158, v158\n\t"
        "v_exp_f32 v159, v159\n\t"
        "s_nop 0\n\t"
        "v_cvt_pk_bf16_f32 v192, v128, v129\n\t"
        "v_add_f32 v224, v224, v144\n\t"
        "v_cvt_pk_bf16_f32 v193, v130, v131\n\t"
        "v_cvt_pk_bf16_f32 v196, v132, v133\n\t"
        "v_cvt_pk_bf16_f32 v197, v134, v135\n\t"
        "v_cvt_pk_bf16_f32 v200, v136, v137\n\t"
        "v_cvt_pk_bf16_f32 v201, v138, v139\n\t"
        "v_cvt_pk_bf16_f32 v204, v140, v141\n\t"
        "v_cvt_pk_bf16_f32 v205, v142, v143\n\t"
        "v_add_f32 v224, v224, v145\n\t"
        "v_add_f32 v224, v224, v146\n\t"
        "v_cvt_pk_bf16_f32 v194, v144, v145\n\t"
        "v_add_f32 v224, v224, v147\n\t"
        "v_add_f32 v226, v226, v148\n\t"
        "v_cvt_pk_bf16_f32 v195, v146, v147\n\t"
        "v_add_f32 v226, v226, v149\n\t"
        "v_cvt_pk_bf16_f32 v198, v148, v149\n\t"
        "v_add_f32 v226, v226, v150\n\t"
        "v_add_f32 v226, v226, v151\n\t"
        "v_add_f32 v228, v228, v152\n\t"
        "v_cvt_pk_bf16_f32 v199, v150, v151\n\t"
        "v_add_f32 v228, v228, v153\n\t"
        "v_add_f32 v228, v228, v154\n\t"
        "v_add_f32 v228, v228, v155\n\t"
        "v_cvt_pk_bf16_f32 v202, v152, v153\n\t"
        "v_cvt_pk_bf16_f32 v203, v154, v155\n\t"
        "v_add_f32 v230, v230, v156\n\t"
        "v_add_f32 v230, v230, v157\n\t"
        "v_cvt_pk_bf16_f32 v206, v156, v157\n\t"
        "v_add_f32 v230, v230, v158\n\t"
        "v_cvt_pk_bf16_f32 v207, v158, v159\n\t"
        "v_add_f32 v230, v230, v159\n\t"
        "v_max3_f32 v250, v224, v226, v228\n\t"
        "v_max_f32 v250, v250, v230\n\t"
        "v_cmp_lt_f32 vcc, s63, v250\n\t"
        "s_cbranch_vccnz RSTUB2_%=\n\t"
        "RRET2_%=:\n\t"
        "v_add_u32 v245, 0x0, v233\n\t"
        "v_add_u32 v246, 0x0, v234\n\t"
        "v_add_u32 v247, 0x0, v251\n\t"
        "v_add_u32 v253, 0x0, v252\n\t"
        "s_mov_b32 s90, 0x18000\n\t"
        "s_branch FIN_%=\n\t"
        "TAIL1_%=:\n\t"
        "v_fma_f32 v188, v188, s56, v231\n\t"
        "v_fma_f32 v189, v189, s56, v231\n\t"
        "v_fma_f32 v190, v190, s56, v231\n\t"
        "v_fma_f32 v191, v191, s56, v231\n\t"
        "v_exp_f32 v176, v176\n\t"
        "v_exp_f32 v177, v177\n\t"
        "v_exp_f32 v178, v178\n\t"
        "v_exp_f32 v179, v179\n\t"
        "v_exp_f32 v180, v180\n\t"
        "v_exp_f32 v181, v181\n\t"
        "v_exp_f32 v182, v182\n\t"
        "v_exp_f32 v183, v183\n\t"
        "v_exp_f32 v184, v184\n\t"
        "v_exp_f32 v185, v185\n\t"
        "v_exp_f32 v186, v186\n\t"
        "v_exp_f32 v187, v187\n\t"
        "v_exp_f32 v188, v188\n\t"
        "v_exp_f32 v189, v189\n\t"
        "v_exp_f32 v190, v190\n\t"
        "v_exp_f32 v191, v191\n\t"
        "s_nop 0\n\t"
        "v_cvt_pk_bf16_f32 v192, v160, v161\n\t"
        "v_add_f32 v224, v224, v176\n\t"
        "v_cvt_pk_bf16_f32 v193, v162, v163\n\t"
        "v_cvt_pk_bf16_f32 v196, v164, v165\n\t"
        "v_cvt_pk_bf16_f32 v197, v166, v167\n\t"
        "v_cvt_pk_bf16_f32 v200, v168, v169\n\t"
        "v_cvt_pk_bf16_f32 v201, v170, v171\n\t"
        "v_cvt_pk_bf16_f32 v204, v172, v173\n\t"
        "v_cvt_pk_bf16_f32 v205, v174, v175\n\t"
        "v_add_f32 v224, v224, v177\n\t"
        "v_add_f32 v224, v224, v178\n\t"
        "v_cvt_pk_bf16_f32 v194, v176, v177\n\t"
        "v_add_f32 v224, v224, v179\n\t"
        "v_add_f32 v226, v226, v180\n\t"
        "v_cvt_pk_bf16_f32 v195, v178, v179\n\t"
        "v_add_f32 v226, v226, v181\n\t"
        "v_cvt_pk_bf16_f32 v198, v180, v181\n\t"
        "v_add_f32 v226, v226, v182\n\t"
        "v_add_f32 v226, v226, v183\n\t"
        "v_add_f32 v228, v228, v184\n\t"
        "v_cvt_pk_bf16_f32 v199, v182, v183\n\t"
        "v_add_f32 v228, v228, v185\n\t"
        "v_add_f32 v228, v228, v186\n\t"
        "v_add_f32 v228, v228, v187\n\t"
        "v_cvt_pk_bf16_f32 v202, v184, v185\n\t"
        "v_cvt_pk_bf16_f32 v203, v186, v187\n\t"
        "v_add_f32 v230, v230, v188\n\t"
        "v_add_f32 v230, v230, v189\n\t"
        "v_cvt_pk_bf16_f32 v206, v188, v189\n\t"
        "v_add_f32 v230, v230, v190\n\t"
        "v_cvt_pk_bf16_f32 v207, v190, v191\n\t"
        "v_add_f32 v230, v230, v191\n\t"
        "v_max3_f32 v250, v224, v226, v228\n\t"
        "v_max_f32 v250, v250, v230\n\t"
        "v_cmp_lt_f32 vcc, s63, v250\n\t"
        "s_cbranch_vccnz RSTUB3_%=\n\t"
        "RRET3_%=:\n\t"
        "v_add_u32 v245, 0x8000, v233\n\t"
        "v_add_u32 v246, 0x8000, v234\n\t"
        "v_add_u32 v247, 0x8000, v251\n\t"
        "v_add_u32 v253, 0x8000, v252\n\t"
        "s_mov_b32 s90, 0x10000\n\t"
        "FIN_%=:\n\t"
        "v_mov_b32 v152, v224\n\t"
        "v_mov_b32 v153, v226\n\t"
        "v_mov_b32 v154, v228\n\t"
        "v_mov_b32 v155, v230\n\t"
        "s_nop 0\n\t"
        "v_permlane16_swap_b32 v224, v152\n\t"
        "v_permlane16_swap_b32 v226, v153\n\t"
        "v_permlane16_swap_b32 v228, v154\n\t"
        "v_permlane16_swap_b32 v230, v155\n\t"
        "s_nop 0\n\t"
        "v_add_f32 v224, v224, v152\n\t"
        "v_add_f32 v226, v226, v153\n\t"
        "v_add_f32 v228, v228, v154\n\t"
        "v_add_f32 v230, v230, v155\n\t"
        "v_mov_b32 v152, v224\n\t"
        "v_mov_b32 v153, v226\n\t"
        "v_mov_b32 v154, v228\n\t"
        "v_mov_b32 v155, v230\n\t"
        "s_nop 0\n\t"
        "v_permlane32_swap_b32 v224, v152\n\t"
        "v_permlane32_swap_b32 v226, v153\n\t"
        "v_permlane32_swap_b32 v228, v154\n\t"
        "v_permlane32_swap_b32 v230, v155\n\t"
        "s_nop 0\n\t"
        "v_add_f32 v224, v224, v152\n\t"
        "v_add_f32 v226, v226, v153\n\t"
        "v_add_f32 v228, v228, v154\n\t"
        "v_add_f32 v230, v230, v155\n\t"
        "s_cmp_lg_u32 s67, 0\n\t"
        "s_cselect_b32 s71, 0x7f800000, 0\n\t"
        "v_or_b32 v224, s71, v224\n\t"
        "v_or_b32 v226, s71, v226\n\t"
        "v_or_b32 v228, s71, v228\n\t"
        "v_or_b32 v230, s71, v230\n\t"
        "v_and_b32 v156, 15, v238\n\t"
        "v_lshlrev_b32 v157, 3, v156\n\t"
        "s_lshl_b32 s91, s70, 9\n\t"
        "s_add_u32 s91, s91, %[lds]\n\t"
        "s_add_u32 s91, s91, 0x27800\n\t"
        "v_add_u32 v242, s91, v157\n\t"
        "v_xor_b32 v243, 0x200, v242\n\t"
        "ds_write_b64 v242, v[224:225] offset:0\n\t"
        "ds_write_b64 v242, v[226:227] offset:128\n\t"
        "ds_write_b64 v242, v[228:229] offset:256\n\t"
        "ds_write_b64 v242, v[230:231] offset:384\n\t"
        "s_waitcnt vmcnt(0) lgkmcnt(0)\n\t"
        "s_barrier\n\t"
        "ds_read_b64 v[128:129], v243 offset:0\n\t"
        "ds_read_b64 v[130:131], v243 offset:128\n\t"
        "ds_read_b64 v[132:133], v243 offset:256\n\t"
        "ds_read_b64 v[134:135], v243 offset:384\n\t"
        "ds_read_b64_tr_b16 v[0:1], v247 offset:2048\n\t"
        "ds_read_b64_tr_b16 v[2:3], v247 offset:10240\n\t"
        "ds_read_b64_tr_b16 v[4:5], v253 offset:2048\n\t"
        "ds_read_b64_tr_b16 v[6:7], v253 offset:10240\n\t"
        "ds_read_b64_tr_b16 v[8:9], v247 offset:2560\n\t"
        "ds_read_b64_tr_b16 v[10:11], v247 offset:10752\n\t"
        "ds_read_b64_tr_b16 v[12:13], v253 offset:2560\n\t"
        "ds_read_b64_tr_b16 v[14:15], v253 offset:10752\n\t"
        "ds_read_b64_tr_b16 v[16:17], v247 offset:3072\n\t"
        "ds_read_b64_tr_b16 v[18:19], v247 offset:11264\n\t"
        "ds_read_b64_tr_b16 v[20:21], v253 offset:3072\n\t"
        "ds_read_b64_tr_b16 v[22:23], v253 offset:11264\n\t"
        "ds_read_b64_tr_b16 v[24:25], v247 offset:3584\n\t"
        "ds_read_b64_tr_b16 v[26:27], v247 offset:11776\n\t"
        "ds_read_b64_tr_b16 v[28:29], v253 offset:3584\n\t"
        "ds_read_b64_tr_b16 v[30:31], v253 offset:11776\n\t"
        "ds_read_b64_tr_b16 v[32:33], v245 offset:0\n\t"
        "ds_read_b64_tr_b16 v[34:35], v245 offset:8192\n\t"
        "ds_read_b64_tr_b16 v[36:37], v246 offset:0\n\t"
        "ds_read_b64_tr_b16 v[38:39], v246 offset:8192\n\t"
        "ds_read_b64_tr_b16 v[40:41], v245 offset:512\n\t"
        "ds_read_b64_tr_b16 v[42:43], v245 offset:8704\n\t"
        "ds_read_b64_tr_b16 v[44:45], v246 offset:512\n\t"
        "ds_read_b64_tr_b16 v[46:47], v246 offset:8704\n\t"
        "ds_read_b64_tr_b16 v[48:49], v245 offset:1024\n\t"
        "ds_read_b64_tr_b16 v[50:51], v245 offset:9216\n\t"
        "ds_read_b64_tr_b16 v[52:53], v246 offset:1024\n\t"
        "ds_read_b64_tr_b16 v[54:55], v246 offset:9216\n\t"
        "ds_read_b64_tr_b16 v[56:57], v245 offset:1536\n\t"
        "ds_read_b64_tr_b16 v[58:59], v245 offset:9728\n\t"
        "ds_read_b64_tr_b16 v[60:61], v246 offset:1536\n\t"
        "ds_read_b64_tr_b16 v[62:63], v246 offset:9728\n\t"
        "s_mov_b64 s[76:77], 0\n\t"
        "s_xor_b32 s71, s70, 0\n\t"
        "s_lshl_b32 s88, s71, 15\n\t"
        "s_cmp_eq_u32 s71, 2\n\t"
        "s_cselect_b32 s88, s90, s88\n\t"
        "s_cmp_eq_u32 s71, 3\n\t"
        "s_cselect_b32 s88, 0x20000, s88\n\t"
        "s_add_u32 s88, s88, %[lds]\n\t"
        "s_xor_b32 s71, s70, 1\n\t"
        "s_lshl_b32 s89, s71, 15\n\t"
        "s_cmp_eq_u32 s71, 2\n\t"
        "s_cselect_b32 s89, s90, s89\n\t"
        "s_cmp_eq_u32 s71, 3\n\t"
        "s_cselect_b32 s89, 0x20000, s89\n\t"
        "s_add_u32 s89, s89, %[lds]\n\t"
        "v_lshlrev_b32 v157, 4, v238\n\t"
        "v_add_u32 v240, s88, v157\n\t"
        "v_add_u32 v241, s89, v157\n\t"
        "s_waitcnt lgkmcnt(15)\n\t"
        "v_mfma_f32_16x16x32_bf16 a[128:131], v[0:3], v[192:195], a[128:131]\n\t"
        "v_min_f32 v152, v225, v129\n\t"
        "v_sub_f32 v153, v152, v225\n\t"
        "v_sub_f32 v154, v152, v129\n\t"
        "s_waitcnt lgkmcnt(15)\n\t"
        "v_mfma_f32_16x16x32_bf16 a[144:147], v[4:7], v[192:195], a[144:147]\n\t"
        "v_exp_f32 v153, v153\n\t"
        "s_nop 0\n\t"
        "v_exp_f32 v154, v154\n\t"
        "s_nop 0\n\t"
        "v_mov_b32 v152, 0x7f800000\n\t"
        "s_waitcnt lgkmcnt(15)\n\t"
        "v_mfma_f32_16x16x32_bf16 a[160:163], v[8:11], v[192:195], a[160:163]\n\t"
        "v_mul_f32 v155, v224, v153\n\t"
        "v_fma_f32 v155, v128, v154, v155\n\t"
        "s_waitcnt lgkmcnt(15)\n\t"
        "v_mfma_f32_16x16x32_bf16 a[176:179], v[12:15], v[192:195], a[176:179]\n\t"
        "v_cmp_nlt_f32 vcc, v155, v152\n\t"
        "v_rcp_f32 v155, v155\n\t"
        "s_nop 0\n\t"
        "s_nop 1\n\t"
        "s_waitcnt lgkmcnt(15)\n\t"
        "v_mfma_f32_16x16x32_bf16 a[192:195], v[16:19], v[192:195], a[192:195]\n\t"
        "s_or_b64 s[76:77], s[76:77], vcc\n\t"
        "v_mul_f32 v136, v153, v155\n\t"
        "v_mul_f32 v144, v154, v155\n\t"
        "s_waitcnt lgkmcnt(15)\n\t"
        "v_mfma_f32_16x16x32_bf16 a[208:211], v[20:23], v[192:195], a[208:211]\n\t"
        "ds_write_b128 v240, a[128:131] offset:0\n\t"
        "v_mov_b32 v137, v136\n\t"
        "v_mov_b32 v145, v144\n\t"
        "v_lshlrev_b32 v244, 9, v156\n\t"
        "s_waitcnt lgkmcnt(15)\n\t"
        "v_mfma_f32_16x16x32_bf16 a[224:227], v[24:27], v[192:195], a[224:227]\n\t"
        "ds_write_b128 v240, a[144:147] offset:1024\n\t"
        "v_bfe_u32 v157, v238, 4, 1\n\t"
        "v_lshl_add_u32 v244, v157, 5, v244\n\t"
        "v_lshrrev_b32 v157, 5, v238\n\t"
        "s_waitcnt lgkmcnt(15)\n\t"
        "v_mfma_f32_16x16x32_bf16 a[240:243], v[28:31], v[192:195], a[240:243]\n\t"
        "ds_write_b128 v240, a[160:163] offset:2048\n\t"
        "v_lshl_add_u32 v244, v157, 4, v244\n\t"
        "s_lshl_b32 s88, s69, 15\n\t"
        "s_waitcnt lgkmcnt(15)\n\t"
        "v_mfma_f32_16x16x32_bf16 a[0:3], v[32:35], v[192:195], a[0:3]\n\t"
        "ds_write_b128 v240, a[176:179] offset:3072\n\t"
        "s_lshl_b32 s89, s68, 8\n\t"
        "s_add_u32 s88, s88, s89\n\t"
        "v_add_u32 v244, s88, v244\n\t"
        "s_waitcnt lgkmcnt(15)\n\t"
        "v_mfma_f32_16x16x32_bf16 a[16:19], v[36:39], v[192:195], a[16:19]\n\t"
        "ds_write_b128 v240, a[192:195] offset:4096\n\t"
        "s_mov_b32 s72, 0\n\t"
        "s_mov_b32 s73, 8192\n\t"
        "s_mov_b32 s74, 16384\n\t"
        "s_waitcnt lgkmcnt(15)\n\t"
        "v_mfma_f32_16x16x32_bf16 a[32:35], v[40:43], v[192:195], a[32:35]\n\t"
        "ds_write_b128 v240, a[208:211] offset:5120\n\t"
        "s_mov_b32 s75, 24576\n\t"
        "v_min_f32 v152, v227, v131\n\t"
        "v_sub_f32 v153, v152, v227\n\t"
        "s_waitcnt lgkmcnt(14)\n\t"
        "v_mfma_f32_16x16x32_bf16 a[48:51], v[44:47], v[192:195], a[48:51]\n\t"
        "ds_write_b128 v240, a[224:227] offset:6144\n\t"
        "v_sub_f32 v154, v152, v131\n\t"
        "v_exp_f32 v153, v153\n\t"
        "s_nop 0\n\t"
        "v_exp_f32 v154, v154\n\t"
        "s_nop 0\n\t"
        "s_waitcnt lgkmcnt(13)\n\t"
        "v_mfma_f32_16x16x32_bf16 a[64:67], v[48:51], v[192:195], a[64:67]\n\t"
        "ds_write_b128 v240, a[240:243] offset:7168\n\t"
        "v_mov_b32 v152, 0x7f800000\n\t"
        "v_mul_f32 v155, v226, v153\n\t"
        "v_fma_f32 v155, v130, v154, v155\n\t"
        "s_waitcnt lgkmcnt(12)\n\t"
        "v_mfma_f32_16x16x32_bf16 a[80:83], v[52:55], v[192:195], a[80:83]\n\t"
        "v_cmp_nlt_f32 vcc, v155, v152\n\t"
        "v_rcp_f32 v155, v155\n\t"
        "s_nop 0\n\t"
        "s_waitcnt lgkmcnt(10)\n\t"
        "v_mfma_f32_16x16x32_bf16 a[96:99], v[56:59], v[192:195], a[96:99]\n\t"
        "s_nop 1\n\t"
        "s_or_b64 s[76:77], s[76:77], vcc\n\t"
        "v_mul_f32 v138, v153, v155\n\t"
        "s_waitcnt lgkmcnt(8)\n\t"
        "v_mfma_f32_16x16x32_bf16 a[112:115], v[60:63], v[192:195], a[112:115]\n\t"
        "v_mul_f32 v146, v154, v155\n\t"
        "v_mov_b32 v139, v138\n\t"
        "v_mov_b32 v147, v146\n\t"
        "s_waitcnt lgkmcnt(0)\n\t"
        "s_barrier\n\t"
        "v_mfma_f32_16x16x32_bf16 a[132:135], v[0:3], v[196:199], a[132:135]\n\t"
        "ds_read_b128 v[64:67], v241 offset:0\n\t"
        "ds_read_b128 v[68:71], v241 offset:1024\n\t"
        "ds_read_b128 v[72:75], v241 offset:2048\n\t"
        "ds_read_b128 v[76:79], v241 offset:3072\n\t"
        "ds_read_b128 v[80:83], v241 offset:4096\n\t"
        "ds_read_b128 v[84:87], v241 offset:5120\n\t"
        "ds_read_b128 v[88:91], v241 offset:6144\n\t"
        "ds_read_b128 v[92:95], v241 offset:7168\n\t"
        "v_mfma_f32_16x16x32_bf16 a[148:151], v[4:7], v[196:199], a[148:151]\n\t"
        "v_accvgpr_read_b32 v160, a0\n\t"
        "v_accvgpr_read_b32 v161, a1\n\t"
        "v_accvgpr_read_b32 v162, a2\n\t"
        "v_accvgpr_read_b32 v163, a3\n\t"
        "v_accvgpr_read_b32 v164, a16\n\t"
        "v_accvgpr_read_b32 v165, a17\n\t"
        "v_accvgpr_read_b32 v166, a18\n\t"
        "v_mfma_f32_16x16x32_bf16 a[164:167], v[8:11], v[196:199], a[164:167]\n\t"
        "v_accvgpr_read_b32 v167, a19\n\t"
        "v_pk_mul_f32 v[160:161], v[160:161], v[136:137]\n\t"
        "v_pk_mul_f32 v[162:163], v[162:163], v[136:137]\n\t"
        "v_pk_mul_f32 v[164:165], v[164:165], v[136:137]\n\t"
        "v_pk_mul_f32 v[166:167], v[166:167], v[136:137]\n\t"
        "s_waitcnt lgkmcnt(6)\n\t"
        "v_pk_fma_f32 v[160:161], v[64:65], v[144:145], v[160:161]\n\t"
        "v_pk_fma_f32 v[162:163], v[66:67], v[144:145], v[162:163]\n\t"
        "v_mfma_f32_16x16x32_bf16 a[180:183], v[12:15], v[196:199], a[180:183]\n\t"
        "v_pk_fma_f32 v[164:165], v[68:69], v[144:145], v[164:165]\n\t"
        "v_pk_fma_f32 v[166:167], v[70:71], v[144:145], v[166:167]\n\t"
        "v_cvt_pk_bf16_f32 v176, v160, v161\n\t"
        "v_cvt_pk_bf16_f32 v177, v162, v163\n\t"
        "v_cvt_pk_bf16_f32 v178, v164, v165\n\t"
        "v_cvt_pk_bf16_f32 v179, v166, v167\n\t"
        "s_nop 1\n\t"
        "v_permlane16_swap_b32 v176, v178\n\t"
        "v_mfma_f32_16x16x32_bf16 a[196:199], v[16:19], v[196:199], a[196:199]\n\t"
        "v_permlane16_swap_b32 v177, v179\n\t"
        "buffer_store_dwordx4 v[176:179], v244, s[48:51], s72 offen offset:0 sc1\n\t"
        "v_accvgpr_read_b32 v168, a32\n\t"
        "v_accvgpr_read_b32 v169, a33\n\t"
        "v_accvgpr_read_b32 v170, a34\n\t"
        "v_accvgpr_read_b32 v171, a35\n\t"
        "v_accvgpr_read_b32 v172, a48\n\t"
        "v_accvgpr_read_b32 v173, a49\n\t"
        "v_mfma_f32_16x16x32_bf16 a[212:215], v[20:23], v[196:199], a[212:215]\n\t"
        "ds_write_b128 v240, a[132:135] offset:8192\n\t"
        "v_accvgpr_read_b32 v174, a50\n\t"
        "v_accvgpr_read_b32 v175, a51\n\t"
        "v_pk_mul_f32 v[168:169], v[168:169], v[136:137]\n\t"
        "v_pk_mul_f32 v[170:171], v[170:171], v[136:137]\n\t"
        "v_pk_mul_f32 v[172:173], v[172:173], v[136:137]\n\t"
        "v_pk_mul_f32 v[174:175], v[174:175], v[136:137]\n\t"
        "s_waitcnt lgkmcnt(5)\n\t"
        "v_pk_fma_f32 v[168:169], v[72:73], v[144:145], v[168:169]\n\t"
        "v_mfma_f32_16x16x32_bf16 a[228:231], v[24:27], v[196:199], a[228:231]\n\t"
        "ds_write_b128 v240, a[148:151] offset:9216\n\t"
        "v_pk_fma_f32 v[170:171], v[74:75], v[144:145], v[170:171]\n\t"
        "v_pk_fma_f32 v[172:173], v[76:77], v[144:145], v[172:173]\n\t"
        "v_pk_fma_f32 v[174:175], v[78:79], v[144:145], v[174:175]\n\t"
        "v_cvt_pk_bf16_f32 v180, v168, v169\n\t"
        "v_cvt_pk_bf16_f32 v181, v170, v171\n\t"
        "v_cvt_pk_bf16_f32 v182, v172, v173\n\t"
        "v_cvt_pk_bf16_f32 v183, v174, v175\n\t"
        "v_mfma_f32_16x16x32_bf16 a[244:247], v[28:31], v[196:199], a[244:247]\n\t"
        "ds_write_b128 v240, a[164:167] offset:10240\n\t"
        "s_nop 1\n\t"
        "v_permlane16_swap_b32 v180, v182\n\t"
        "v_permlane16_swap_b32 v181, v183\n\t"
        "buffer_store_dwordx4 v[180:183], v244, s[48:51], s72 offen offset:64 sc1\n\t"
        "v_accvgpr_read_b32 v160, a64\n\t"
        "v_accvgpr_read_b32 v161, a65\n\t"
        "v_accvgpr_read_b32 v162, a66\n\t"
        "v_accvgpr_read_b32 v163, a67\n\t"
        "v_mfma_f32_16x16x32_bf16 a[4:7], v[32:35], v[196:199], a[4:7]\n\t"
        "ds_write_b128 v240, a[180:183] offset:11264\n\t"
        "v_accvgpr_read_b32 v164, a80\n\t"
        "v_accvgpr_read_b32 v165, a81\n\t"
        "v_accvgpr_read_b32 v166, a82\n\t"
        "v_accvgpr_read_b32 v167, a83\n\t"
        "v_pk_mul_f32 v[160:161], v[160:161], v[136:137]\n\t"
        "v_pk_mul_f32 v[162:163], v[162:163], v[136:137]\n\t"
        "v_pk_mul_f32 v[164:165], v[164:165], v[136:137]\n\t"
        "v_pk_mul_f32 v[166:167], v[166:167], v[136:137]\n\t"
        "v_mfma_f32_16x16x32_bf16 a[20:23], v[36:39], v[196:199], a[20:23]\n\t"
        "ds_write_b128 v240, a[196:199] offset:12288\n\t"
        "s_waitcnt lgkmcnt(7)\n\t"
        "v_pk_fma_f32 v[160:161], v[80:81], v[144:145], v[160:161]\n\t"
        "v_pk_fma_f32 v[162:163], v[82:83], v[144:145], v[162:163]\n\t"
        "v_pk_fma_f32 v[164:165], v[84:85], v[144:145], v[164:165]\n\t"
        "v_pk_fma_f32 v[166:167], v[86:87], v[144:145], v[166:167]\n\t"
        "v_cvt_pk_bf16_f32 v184, v160, v161\n\t"
        "v_cvt_pk_bf16_f32 v185, v162, v163\n\t"
        "v_cvt_pk_bf16_f32 v186, v164, v165\n\t"
        "v_mfma_f32_16x16x32_bf16 a[36:39], v[40:43], v[196:199], a[36:39]\n\t"
        "ds_write_b128 v240, a[212:215] offset:13312\n\t"
        "v_cvt_pk_bf16_f32 v187, v166, v167\n\t"
        "s_nop 1\n\t"
        "v_permlane16_swap_b32 v184, v186\n\t"
        "v_permlane16_swap_b32 v185, v187\n\t"
        "buffer_store_dwordx4 v[184:187], v244, s[48:51], s72 offen offset:128 sc1\n\t"
        "v_accvgpr_read_b32 v168, a96\n\t"
        "v_accvgpr_read_b32 v169, a97\n\t"
        "v_mfma_f32_16x16x32_bf16 a[52:55], v[44:47], v[196:199], a[52:55]\n\t"
        "ds_write_b128 v240, a[228:231] offset:14336\n\t"
        "v_accvgpr_read_b32 v170, a98\n\t"
        "v_accvgpr_read_b32 v171, a99\n\t"
        "v_accvgpr_read_b32 v172, a112\n\t"
        "v_accvgpr_read_b32 v173, a113\n\t"
        "v_accvgpr_read_b32 v174, a114\n\t"
        "v_accvgpr_read_b32 v175, a115\n\t"
        "v_pk_mul_f32 v[168:169], v[168:169], v[136:137]\n\t"
        "v_pk_mul_f32 v[170:171], v[170:171], v[136:137]\n\t"
        "v_mfma_f32_16x16x32_bf16 a[68:71], v[48:51], v[196:199], a[68:71]\n\t"
        "ds_write_b128 v240, a[244:247] offset:15360\n\t"
        "v_pk_mul_f32 v[172:173], v[172:173], v[136:137]\n\t"
        "v_pk_mul_f32 v[174:175], v[174:175], v[136:137]\n\t"
        "s_waitcnt lgkmcnt(8)\n\t"
        "v_pk_fma_f32 v[168:169], v[88:89], v[144:145], v[168:169]\n\t"
        "v_pk_fma_f32 v[170:171], v[90:91], v[144:145], v[170:171]\n\t"
        "v_pk_fma_f32 v[172:173], v[92:93], v[144:145], v[172:173]\n\t"
        "v_pk_fma_f32 v[174:175], v[94:95], v[144:145], v[174:175]\n\t"
        "v_cvt_pk_bf16_f32 v188, v168, v169\n\t"
        "v_cvt_pk_bf16_f32 v189, v170, v171\n\t"
        "v_mfma_f32_16x16x32_bf16 a[84:87], v[52:55], v[196:199], a[84:87]\n\t"
        "v_cvt_pk_bf16_f32 v190, v172, v173\n\t"
        "v_cvt_pk_bf16_f32 v191, v174, v175\n\t"
        "s_nop 1\n\t"
        "v_permlane16_swap_b32 v188, v190\n\t"
        "v_permlane16_swap_b32 v189, v191\n\t"
        "buffer_store_dwordx4 v[188:191], v244, s[48:51], s72 offen offset:192 sc1\n\t"
        "v_min_f32 v152, v229, v133\n\t"
        "v_mfma_f32_16x16x32_bf16 a[100:103], v[56:59], v[196:199], a[100:103]\n\t"
        "v_sub_f32 v153, v152, v229\n\t"
        "v_sub_f32 v154, v152, v133\n\t"
        "v_exp_f32 v153, v153\n\t"
        "s_nop 0\n\t"
        "v_exp_f32 v154, v154\n\t"
        "s_nop 0\n\t"
        "v_mov_b32 v152, 0x7f800000\n\t"
        "v_mul_f32 v155, v228, v153\n\t"
        "v_fma_f32 v155, v132, v154, v155\n\t"
        "v_mfma_f32_16x16x32_bf16 a[116:119], v[60:63], v[196:199], a[116:119]\n\t"
        "v_cmp_nlt_f32 vcc, v155, v152\n\t"
        "v_rcp_f32 v155, v155\n\t"
        "s_nop 0\n\t"
        "s_nop 1\n\t"
        "s_or_b64 s[76:77], s[76:77], vcc\n\t"
        "v_mul_f32 v140, v153, v155\n\t"
        "v_mul_f32 v148, v154, v155\n\t"
        "v_mov_b32 v141, v140\n\t"
        "v_mov_b32 v149, v148\n\t"
        "s_waitcnt lgkmcnt(0)\n\t"
        "s_barrier\n\t"
        "v_mfma_f32_16x16x32_bf16 a[136:139], v[0:3], v[200:203], a[136:139]\n\t"
        "ds_read_b128 v[96:99], v241 offset:8192\n\t"
        "ds_read_b128 v[100:103], v241 offset:9216\n\t"
        "ds_read_b128 v[104:107], v241 offset:10240\n\t"
        "ds_read_b128 v[108:111], v241 offset:11264\n\t"
        "ds_read_b128 v[112:115], v241 offset:12288\n\t"
        "ds_read_b128 v[116:119], v241 offset:13312\n\t"
        "ds_read_b128 v[120:123], v241 offset:14336\n\t"
        "ds_read_b128 v[124:127], v241 offset:15360\n\t"
        "v_mfma_f32_16x16x32_bf16 a[152:155], v[4:7], v[200:203], a[152:155]\n\t"
        "v_accvgpr_read_b32 v160, a4\n\t"
        "v_accvgpr_read_b32 v161, a5\n\t"
        "v_accvgpr_read_b32 v162, a6\n\t"
        "v_accvgpr_read_b32 v163, a7\n\t"
        "v_accvgpr_read_b32 v164, a20\n\t"
        "v_accvgpr_read_b32 v165, a21\n\t"
        "v_accvgpr_read_b32 v166, a22\n\t"
        "v_mfma_f32_16x16x32_bf16 a[168:171], v[8:11], v[200:203], a[168:171]\n\t"
        "v_accvgpr_read_b32 v167, a23\n\t"
        "v_pk_mul_f32 v[160:161], v[160:161], v[138:139]\n\t"
        "v_pk_mul_f32 v[162:163], v[162:163], v[138:139]\n\t"
        "v_pk_mul_f32 v[164:165], v[164:165], v[138:139]\n\t"
        "v_pk_mul_f32 v[166:167], v[166:167], v[138:139]\n\t"
        "s_waitcnt lgkmcnt(6)\n\t"
        "v_pk_fma_f32 v[160:161], v[96:97], v[146:147], v[160:161]\n\t"
        "v_pk_fma_f32 v[162:163], v[98:99], v[146:147], v[162:163]\n\t"
        "v_mfma_f32_16x16x32_bf16 a[184:187], v[12:15], v[200:203], a[184:187]\n\t"
        "v_pk_fma_f32 v[164:165], v[100:101], v[146:147], v[164:165]\n\t"
        "v_pk_fma_f32 v[166:167], v[102:103], v[146:147], v[166:167]\n\t"
        "v_cvt_pk_bf16_f32 v176, v160, v161\n\t"
        "v_cvt_pk_bf16_f32 v177, v162, v163\n\t"
        "v_cvt_pk_bf16_f32 v178, v164, v165\n\t"
        "v_cvt_pk_bf16_f32 v179, v166, v167\n\t"
        "s_nop 1\n\t"
        "v_permlane16_swap_b32 v176, v178\n\t"
        "v_mfma_f32_16x16x32_bf16 a[200:203], v[16:19], v[200:203], a[200:203]\n\t"
        "v_permlane16_swap_b32 v177, v179\n\t"
        "buffer_store_dwordx4 v[176:179], v244, s[48:51], s73 offen offset:0 sc1\n\t"
        "v_accvgpr_read_b32 v168, a36\n\t"
        "v_accvgpr_read_b32 v169, a37\n\t"
        "v_accvgpr_read_b32 v170, a38\n\t"
        "v_accvgpr_read_b32 v171, a39\n\t"
        "v_accvgpr_read_b32 v172, a52\n\t"
        "v_accvgpr_read_b32 v173, a53\n\t"
        "v_mfma_f32_16x16x32_bf16 a[216:219], v[20:23], v[200:203], a[216:219]\n\t"
        "ds_write_b128 v240, a[136:139] offset:16384\n\t"
        "v_accvgpr_read_b32 v174, a54\n\t"
        "v_accvgpr_read_b32 v175, a55\n\t"
        "v_pk_mul_f32 v[168:169], v[168:169], v[138:139]\n\t"
        "v_pk_mul_f32 v[170:171], v[170:171], v[138:139]\n\t"
        "v_pk_mul_f32 v[172:173], v[172:173], v[138:139]\n\t"
        "v_pk_mul_f32 v[174:175], v[174:175], v[138:139]\n\t"
        "s_waitcnt lgkmcnt(5)\n\t"
        "v_pk_fma_f32 v[168:169], v[104:105], v[146:147], v[168:169]\n\t"
        "v_mfma_f32_16x16x32_bf16 a[232:235], v[24:27], v[200:203], a[232:235]\n\t"
        "ds_write_b128 v240, a[152:155] offset:17408\n\t"
        "v_pk_fma_f32 v[170:171], v[106:107], v[146:147], v[170:171]\n\t"
        "v_pk_fma_f32 v[172:173], v[108:109], v[146:147], v[172:173]\n\t"
        "v_pk_fma_f32 v[174:175], v[110:111], v[146:147], v[174:175]\n\t"
        "v_cvt_pk_bf16_f32 v180, v168, v169\n\t"
        "v_cvt_pk_bf16_f32 v181, v170, v171\n\t"
        "v_cvt_pk_bf16_f32 v182, v172, v173\n\t"
        "v_cvt_pk_bf16_f32 v183, v174, v175\n\t"
        "v_mfma_f32_16x16x32_bf16 a[248:251], v[28:31], v[200:203], a[248:251]\n\t"
        "ds_write_b128 v240, a[168:171] offset:18432\n\t"
        "s_nop 1\n\t"
        "v_permlane16_swap_b32 v180, v182\n\t"
        "v_permlane16_swap_b32 v181, v183\n\t"
        "buffer_store_dwordx4 v[180:183], v244, s[48:51], s73 offen offset:64 sc1\n\t"
        "v_accvgpr_read_b32 v160, a68\n\t"
        "v_accvgpr_read_b32 v161, a69\n\t"
        "v_accvgpr_read_b32 v162, a70\n\t"
        "v_accvgpr_read_b32 v163, a71\n\t"
        "v_mfma_f32_16x16x32_bf16 a[8:11], v[32:35], v[200:203], a[8:11]\n\t"
        "ds_write_b128 v240, a[184:187] offset:19456\n\t"
        "v_accvgpr_read_b32 v164, a84\n\t"
        "v_accvgpr_read_b32 v165, a85\n\t"
        "v_accvgpr_read_b32 v166, a86\n\t"
        "v_accvgpr_read_b32 v167, a87\n\t"
        "v_pk_mul_f32 v[160:161], v[160:161], v[138:139]\n\t"
        "v_pk_mul_f32 v[162:163], v[162:163], v[138:139]\n\t"
        "v_pk_mul_f32 v[164:165], v[164:165], v[138:139]\n\t"
        "v_pk_mul_f32 v[166:167], v[166:167], v[138:139]\n\t"
        "v_mfma_f32_16x16x32_bf16 a[24:27], v[36:39], v[200:203], a[24:27]\n\t"
        "ds_write_b128 v240, a[200:203] offset:20480\n\t"
        "s_waitcnt lgkmcnt(7)\n\t"
        "v_pk_fma_f32 v[160:161], v[112:113], v[146:147], v[160:161]\n\t"
        "v_pk_fma_f32 v[162:163], v[114:115], v[146:147], v[162:163]\n\t"
        "v_pk_fma_f32 v[164:165], v[116:117], v[146:147], v[164:165]\n\t"
        "v_pk_fma_f32 v[166:167], v[118:119], v[146:147], v[166:167]\n\t"
        "v_cvt_pk_bf16_f32 v184, v160, v161\n\t"
        "v_cvt_pk_bf16_f32 v185, v162, v163\n\t"
        "v_cvt_pk_bf16_f32 v186, v164, v165\n\t"
        "v_mfma_f32_16x16x32_bf16 a[40:43], v[40:43], v[200:203], a[40:43]\n\t"
        "ds_write_b128 v240, a[216:219] offset:21504\n\t"
        "v_cvt_pk_bf16_f32 v187, v166, v167\n\t"
        "s_nop 1\n\t"
        "v_permlane16_swap_b32 v184, v186\n\t"
        "v_permlane16_swap_b32 v185, v187\n\t"
        "buffer_store_dwordx4 v[184:187], v244, s[48:51], s73 offen offset:128 sc1\n\t"
        "v_accvgpr_read_b32 v168, a100\n\t"
        "v_accvgpr_read_b32 v169, a101\n\t"
        "v_mfma_f32_16x16x32_bf16 a[56:59], v[44:47], v[200:203], a[56:59]\n\t"
        "ds_write_b128 v240, a[232:235] offset:22528\n\t"
        "v_accvgpr_read_b32 v170, a102\n\t"
        "v_accvgpr_read_b32 v171, a103\n\t"
        "v_accvgpr_read_b32 v172, a116\n\t"
        "v_accvgpr_read_b32 v173, a117\n\t"
        "v_accvgpr_read_b32 v174, a118\n\t"
        "v_accvgpr_read_b32 v175, a119\n\t"
        "v_pk_mul_f32 v[168:169], v[168:169], v[138:139]\n\t"
        "v_pk_mul_f32 v[170:171], v[170:171], v[138:139]\n\t"
        "v_mfma_f32_16x16x32_bf16 a[72:75], v[48:51], v[200:203], a[72:75]\n\t"
        "ds_write_b128 v240, a[248:251] offset:23552\n\t"
        "v_pk_mul_f32 v[172:173], v[172:173], v[138:139]\n\t"
        "v_pk_mul_f32 v[174:175], v[174:175], v[138:139]\n\t"
        "s_waitcnt lgkmcnt(8)\n\t"
        "v_pk_fma_f32 v[168:169], v[120:121], v[146:147], v[168:169]\n\t"
        "v_pk_fma_f32 v[170:171], v[122:123], v[146:147], v[170:171]\n\t"
        "v_pk_fma_f32 v[172:173], v[124:125], v[146:147], v[172:173]\n\t"
        "v_pk_fma_f32 v[174:175], v[126:127], v[146:147], v[174:175]\n\t"
        "v_cvt_pk_bf16_f32 v188, v168, v169\n\t"
        "v_cvt_pk_bf16_f32 v189, v170, v171\n\t"
        "v_mfma_f32_16x16x32_bf16 a[88:91], v[52:55], v[200:203], a[88:91]\n\t"
        "v_cvt_pk_bf16_f32 v190, v172, v173\n\t"
        "v_cvt_pk_bf16_f32 v191, v174, v175\n\t"
        "s_nop 1\n\t"
        "v_permlane16_swap_b32 v188, v190\n\t"
        "v_permlane16_swap_b32 v189, v191\n\t"
        "buffer_store_dwordx4 v[188:191], v244, s[48:51], s73 offen offset:192 sc1\n\t"
        "v_min_f32 v152, v231, v135\n\t"
        "v_mfma_f32_16x16x32_bf16 a[104:107], v[56:59], v[200:203], a[104:107]\n\t"
        "v_sub_f32 v153, v152, v231\n\t"
        "v_sub_f32 v154, v152, v135\n\t"
        "v_exp_f32 v153, v153\n\t"
        "s_nop 0\n\t"
        "v_exp_f32 v154, v154\n\t"
        "s_nop 0\n\t"
        "v_mov_b32 v152, 0x7f800000\n\t"
        "v_mul_f32 v155, v230, v153\n\t"
        "v_fma_f32 v155, v134, v154, v155\n\t"
        "v_mfma_f32_16x16x32_bf16 a[120:123], v[60:63], v[200:203], a[120:123]\n\t"
        "v_cmp_nlt_f32 vcc, v155, v152\n\t"
        "v_rcp_f32 v155, v155\n\t"
        "s_nop 0\n\t"
        "s_nop 1\n\t"
        "s_or_b64 s[76:77], s[76:77], vcc\n\t"
        "v_mul_f32 v142, v153, v155\n\t"
        "v_mul_f32 v150, v154, v155\n\t"
        "v_mov_b32 v143, v142\n\t"
        "v_mov_b32 v151, v150\n\t"
        "s_waitcnt lgkmcnt(0)\n\t"
        "s_barrier\n\t"
        "v_mfma_f32_16x16x32_bf16 a[140:143], v[0:3], v[204:207], a[140:143]\n\t"
        "ds_read_b128 v[64:67], v241 offset:16384\n\t"
        "ds_read_b128 v[68:71], v241 offset:17408\n\t"
        "ds_read_b128 v[72:75], v241 offset:18432\n\t"
        "ds_read_b128 v[76:79], v241 offset:19456\n\t"
        "ds_read_b128 v[80:83], v241 offset:20480\n\t"
        "ds_read_b128 v[84:87], v241 offset:21504\n\t"
        "v_mfma_f32_16x16x32_bf16 a[156:159], v[4:7], v[204:207], a[156:159]\n\t"
        "ds_read_b128 v[88:91], v241 offset:22528\n\t"
        "ds_read_b128 v[92:95], v241 offset:23552\n\t"
        "v_accvgpr_read_b32 v160, a8\n\t"
        "v_accvgpr_read_b32 v161, a9\n\t"
        "v_accvgpr_read_b32 v162, a10\n\t"
        "v_accvgpr_read_b32 v163, a11\n\t"
        "v_accvgpr_read_b32 v164, a24\n\t"
        "v_mfma_f32_16x16x32_bf16 a[172:175], v[8:11], v[204:207], a[172:175]\n\t"
        "v_accvgpr_read_b32 v165, a25\n\t"
        "v_accvgpr_read_b32 v166, a26\n\t"
        "v_accvgpr_read_b32 v167, a27\n\t"
        "v_pk_mul_f32 v[160:161], v[160:161], v[140:141]\n\t"
        "v_pk_mul_f32 v[162:163], v[162:163], v[140:141]\n\t"
        "v_pk_mul_f32 v[164:165], v[164:165], v[140:141]\n\t"
        "v_pk_mul_f32 v[166:167], v[166:167], v[140:141]\n\t"
        "v_mfma_f32_16x16x32_bf16 a[188:191], v[12:15], v[204:207], a[188:191]\n\t"
        "s_waitcnt lgkmcnt(6)\n\t"
        "v_pk_fma_f32 v[160:161], v[64:65], v[148:149], v[160:161]\n\t"
        "v_pk_fma_f32 v[162:163], v[66:67], v[148:149], v[162:163]\n\t"
        "v_pk_fma_f32 v[164:165], v[68:69], v[148:149], v[164:165]\n\t"
        "v_pk_fma_f32 v[166:167], v[70:71], v[148:149], v[166:167]\n\t"
        "v_cvt_pk_bf16_f32 v176, v160, v161\n\t"
        "v_cvt_pk_bf16_f32 v177, v162, v163\n\t"
        "v_mfma_f32_16x16x32_bf16 a[204:207], v[16:19], v[204:207], a[204:207]\n\t"
        "v_cvt_pk_bf16_f32 v178, v164, v165\n\t"
        "v_cvt_pk_bf16_f32 v179, v166, v167\n\t"
        "s_nop 1\n\t"
        "v_permlane16_swap_b32 v176, v178\n\t"
        "v_permlane16_swap_b32 v177, v179\n\t"
        "buffer_store_dwordx4 v[176:179], v244, s[48:51], s74 offen offset:0 sc1\n\t"
        "v_mfma_f32_16x16x32_bf16 a[220:223], v[20:23], v[204:207], a[220:223]\n\t"
        "ds_write_b128 v240, a[140:143] offset:24576\n\t"
        "v_accvgpr_read_b32 v168, a40\n\t"
        "v_accvgpr_read_b32 v169, a41\n\t"
        "v_accvgpr_read_b32 v170, a42\n\t"
        "v_accvgpr_read_b32 v171, a43\n\t"
        "v_accvgpr_read_b32 v172, a56\n\t"
        "v_accvgpr_read_b32 v173, a57\n\t"
        "v_accvgpr_read_b32 v174, a58\n\t"
        "v_mfma_f32_16x16x32_bf16 a[236:239], v[24:27], v[204:207], a[236:239]\n\t"
        "ds_write_b128 v240, a[156:159] offset:25600\n\t"
        "v_accvgpr_read_b32 v175, a59\n\t"
        "v_pk_mul_f32 v[168:169], v[168:169], v[140:141]\n\t"
        "v_pk_mul_f32 v[170:171], v[170:171], v[140:141]\n\t"
        "v_pk_mul_f32 v[172:173], v[172:173], v[140:141]\n\t"
        "v_pk_mul_f32 v[174:175], v[174:175], v[140:141]\n\t"
        "s_waitcnt lgkmcnt(6)\n\t"
        "v_pk_fma_f32 v[168:169], v[72:73], v[148:149], v[168:169]\n\t"
        "v_pk_fma_f32 v[170:171], v[74:75], v[148:149], v[170:171]\n\t"
        "v_mfma_f32_16x16x32_bf16 a[252:255], v[28:31], v[204:207], a[252:255]\n\t"
        "ds_write_b128 v240, a[172:175] offset:26624\n\t"
        "v_pk_fma_f32 v[172:173], v[76:77], v[148:149], v[172:173]\n\t"
        "v_pk_fma_f32 v[174:175], v[78:79], v[148:149], v[174:175]\n\t"
        "v_cvt_pk_bf16_f32 v180, v168, v169\n\t"
        "v_cvt_pk_bf16_f32 v181, v170, v171\n\t"
        "v_cvt_pk_bf16_f32 v182, v172, v173\n\t"
        "v_cvt_pk_bf16_f32 v183, v174, v175\n\t"
        "v_mfma_f32_16x16x32_bf16 a[12:15], v[32:35], v[204:207], a[12:15]\n\t"
        "ds_write_b128 v240, a[188:191] offset:27648\n\t"
        "s_nop 1\n\t"
        "v_permlane16_swap_b32 v180, v182\n\t"
        "v_permlane16_swap_b32 v181, v183\n\t"
        "buffer_store_dwordx4 v[180:183], v244, s[48:51], s74 offen offset:64 sc1\n\t"
        "v_accvgpr_read_b32 v160, a72\n\t"
        "v_accvgpr_read_b32 v161, a73\n\t"
        "v_mfma_f32_16x16x32_bf16 a[28:31], v[36:39], v[204:207], a[28:31]\n\t"
        "ds_write_b128 v240, a[204:207] offset:28672\n\t"
        "v_accvgpr_read_b32 v162, a74\n\t"
        "v_accvgpr_read_b32 v163, a75\n\t"
        "v_accvgpr_read_b32 v164, a88\n\t"
        "v_accvgpr_read_b32 v165, a89\n\t"
        "v_accvgpr_read_b32 v166, a90\n\t"
        "v_accvgpr_read_b32 v167, a91\n\t"
        "v_pk_mul_f32 v[160:161], v[160:161], v[140:141]\n\t"
        "v_mfma_f32_16x16x32_bf16 a[44:47], v[40:43], v[204:207], a[44:47]\n\t"
        "ds_write_b128 v240, a[220:223] offset:29696\n\t"
        "v_pk_mul_f32 v[162:163], v[162:163], v[140:141]\n\t"
        "v_pk_mul_f32 v[164:165], v[164:165], v[140:141]\n\t"
        "v_pk_mul_f32 v[166:167], v[166:167], v[140:141]\n\t"
        "s_waitcnt lgkmcnt(8)\n\t"
        "v_pk_fma_f32 v[160:161], v[80:81], v[148:149], v[160:161]\n\t"
        "v_pk_fma_f32 v[162:163], v[82:83], v[148:149], v[162:163]\n\t"
        "v_pk_fma_f32 v[164:165], v[84:85], v[148:149], v[164:165]\n\t"
        "v_pk_fma_f32 v[166:167], v[86:87], v[148:149], v[166:167]\n\t"
        "v_mfma_f32_16x16x32_bf16 a[60:63], v[44:47], v[204:207], a[60:63]\n\t"
        "ds_write_b128 v240, a[236:239] offset:30720\n\t"
        "v_cvt_pk_bf16_f32 v184, v160, v161\n\t"
        "v_cvt_pk_bf16_f32 v185, v162, v163\n\t"
        "v_cvt_pk_bf16_f32 v186, v164, v165\n\t"
        "v_cvt_pk_bf16_f32 v187, v166, v167\n\t"
        "s_nop 1\n\t"
        "v_permlane16_swap_b32 v184, v186\n\t"
        "v_mfma_f32_16x16x32_bf16 a[76:79], v[48:51], v[204:207], a[76:79]\n\t"
        "ds_write_b128 v240, a[252:255] offset:31744\n\t"
        "v_permlane16_swap_b32 v185, v187\n\t"
        "buffer_store_dwordx4 v[184:187], v244, s[48:51], s74 offen offset:128 sc1\n\t"
        "v_accvgpr_read_b32 v168, a104\n\t"
        "v_accvgpr_read_b32 v169, a105\n\t"
        "v_accvgpr_read_b32 v170, a106\n\t"
        "v_accvgpr_read_b32 v171, a107\n\t"
        "v_mfma_f32_16x16x32_bf16 a[92:95], v[52:55], v[204:207], a[92:95]\n\t"
        "v_accvgpr_read_b32 v172, a120\n\t"
        "v_accvgpr_read_b32 v173, a121\n\t"
        "v_accvgpr_read_b32 v174, a122\n\t"
        "v_accvgpr_read_b32 v175, a123\n\t"
        "v_pk_mul_f32 v[168:169], v[168:169], v[140:141]\n\t"
        "v_pk_mul_f32 v[170:171], v[170:171], v[140:141]\n\t"
        "v_pk_mul_f32 v[172:173], v[172:173], v[140:141]\n\t"
        "v_mfma_f32_16x16x32_bf16 a[108:111], v[56:59], v[204:207], a[108:111]\n\t"
        "v_pk_mul_f32 v[174:175], v[174:175], v[140:141]\n\t"
        "s_waitcnt lgkmcnt(8)\n\t"
        "v_pk_fma_f32 v[168:169], v[88:89], v[148:149], v[168:169]\n\t"
        "v_pk_fma_f32 v[170:171], v[90:91], v[148:149], v[170:171]\n\t"
        "v_pk_fma_f32 v[172:173], v[92:93], v[148:149], v[172:173]\n\t"
        "v_pk_fma_f32 v[174:175], v[94:95], v[148:149], v[174:175]\n\t"
        "v_cvt_pk_bf16_f32 v188, v168, v169\n\t"
        "v_cvt_pk_bf16_f32 v189, v170, v171\n\t"
        "v_mfma_f32_16x16x32_bf16 a[124:127], v[60:63], v[204:207], a[124:127]\n\t"
        "v_cvt_pk_bf16_f32 v190, v172, v173\n\t"
        "v_cvt_pk_bf16_f32 v191, v174, v175\n\t"
        "s_nop 1\n\t"
        "v_permlane16_swap_b32 v188, v190\n\t"
        "v_permlane16_swap_b32 v189, v191\n\t"
        "buffer_store_dwordx4 v[188:191], v244, s[48:51], s74 offen offset:192 sc1\n\t"
        "s_cmp_lg_u64 s[76:77], 0\n\t"
        "s_cselect_b32 s71, 1, 0\n\t"
        "s_or_b32 s67, s67, s71\n\t"
        "s_waitcnt lgkmcnt(0)\n\t"
        "s_barrier\n\t"
        "ds_read_b128 v[96:99], v241 offset:24576\n\t"
        "ds_read_b128 v[100:103], v241 offset:25600\n\t"
        "ds_read_b128 v[104:107], v241 offset:26624\n\t"
        "ds_read_b128 v[108:111], v241 offset:27648\n\t"
        "ds_read_b128 v[112:115], v241 offset:28672\n\t"
        "ds_read_b128 v[116:119], v241 offset:29696\n\t"
        "ds_read_b128 v[120:123], v241 offset:30720\n\t"
        "ds_read_b128 v[124:127], v241 offset:31744\n\t"
        "v_accvgpr_read_b32 v160, a12\n\t"
        "v_accvgpr_read_b32 v161, a13\n\t"
        "v_accvgpr_read_b32 v162, a14\n\t"
        "v_accvgpr_read_b32 v163, a15\n\t"
        "v_accvgpr_read_b32 v164, a28\n\t"
        "v_accvgpr_read_b32 v165, a29\n\t"
        "v_accvgpr_read_b32 v166, a30\n\t"
        "v_accvgpr_read_b32 v167, a31\n\t"
        "v_pk_mul_f32 v[160:161], v[160:161], v[142:143]\n\t"
        "v_pk_mul_f32 v[162:163], v[162:163], v[142:143]\n\t"
        "v_pk_mul_f32 v[164:165], v[164:165], v[142:143]\n\t"
        "v_pk_mul_f32 v[166:167], v[166:167], v[142:143]\n\t"
        "s_waitcnt lgkmcnt(6)\n\t"
        "v_pk_fma_f32 v[160:161], v[96:97], v[150:151], v[160:161]\n\t"
        "v_pk_fma_f32 v[162:163], v[98:99], v[150:151], v[162:163]\n\t"
        "v_pk_fma_f32 v[164:165], v[100:101], v[150:151], v[164:165]\n\t"
        "v_pk_fma_f32 v[166:167], v[102:103], v[150:151], v[166:167]\n\t"
        "v_cvt_pk_bf16_f32 v176, v160, v161\n\t"
        "v_cvt_pk_bf16_f32 v177, v162, v163\n\t"
        "v_cvt_pk_bf16_f32 v178, v164, v165\n\t"
        "v_cvt_pk_bf16_f32 v179, v166, v167\n\t"
        "s_nop 1\n\t"
        "v_permlane16_swap_b32 v176, v178\n\t"
        "v_permlane16_swap_b32 v177, v179\n\t"
        "buffer_store_dwordx4 v[176:179], v244, s[48:51], s75 offen offset:0 sc1\n\t"
        "v_accvgpr_read_b32 v168, a44\n\t"
        "v_accvgpr_read_b32 v169, a45\n\t"
        "v_accvgpr_read_b32 v170, a46\n\t"
        "v_accvgpr_read_b32 v171, a47\n\t"
        "v_accvgpr_read_b32 v172, a60\n\t"
        "v_accvgpr_read_b32 v173, a61\n\t"
        "v_accvgpr_read_b32 v174, a62\n\t"
        "v_accvgpr_read_b32 v175, a63\n\t"
        "v_pk_mul_f32 v[168:169], v[168:169], v[142:143]\n\t"
        "v_pk_mul_f32 v[170:171], v[170:171], v[142:143]\n\t"
        "v_pk_mul_f32 v[172:173], v[172:173], v[142:143]\n\t"
        "v_pk_mul_f32 v[174:175], v[174:175], v[142:143]\n\t"
        "s_waitcnt lgkmcnt(4)\n\t"
        "v_pk_fma_f32 v[168:169], v[104:105], v[150:151], v[168:169]\n\t"
        "v_pk_fma_f32 v[170:171], v[106:107], v[150:151], v[170:171]\n\t"
        "v_pk_fma_f32 v[172:173], v[108:109], v[150:151], v[172:173]\n\t"
        "v_pk_fma_f32 v[174:175], v[110:111], v[150:151], v[174:175]\n\t"
        "v_cvt_pk_bf16_f32 v180, v168, v169\n\t"
        "v_cvt_pk_bf16_f32 v181, v170, v171\n\t"
        "v_cvt_pk_bf16_f32 v182, v172, v173\n\t"
        "v_cvt_pk_bf16_f32 v183, v174, v175\n\t"
        "s_nop 1\n\t"
        "v_permlane16_swap_b32 v180, v182\n\t"
        "v_permlane16_swap_b32 v181, v183\n\t"
        "buffer_store_dwordx4 v[180:183], v244, s[48:51], s75 offen offset:64 sc1\n\t"
        "v_accvgpr_read_b32 v160, a76\n\t"
        "v_accvgpr_read_b32 v161, a77\n\t"
        "v_accvgpr_read_b32 v162, a78\n\t"
        "v_accvgpr_read_b32 v163, a79\n\t"
        "v_accvgpr_read_b32 v164, a92\n\t"
        "v_accvgpr_read_b32 v165, a93\n\t"
        "v_accvgpr_read_b32 v166, a94\n\t"
        "v_accvgpr_read_b32 v167, a95\n\t"
        "v_pk_mul_f32 v[160:161], v[160:161], v[142:143]\n\t"
        "v_pk_mul_f32 v[162:163], v[162:163], v[142:143]\n\t"
        "v_pk_mul_f32 v[164:165], v[164:165], v[142:143]\n\t"
        "v_pk_mul_f32 v[166:167], v[166:167], v[142:143]\n\t"
        "s_waitcnt lgkmcnt(2)\n\t"
        "v_pk_fma_f32 v[160:161], v[112:113], v[150:151], v[160:161]\n\t"
        "v_pk_fma_f32 v[162:163], v[114:115], v[150:151], v[162:163]\n\t"
        "v_pk_fma_f32 v[164:165], v[116:117], v[150:151], v[164:165]\n\t"
        "v_pk_fma_f32 v[166:167], v[118:119], v[150:151], v[166:167]\n\t"
        "v_cvt_pk_bf16_f32 v184, v160, v161\n\t"
        "v_cvt_pk_bf16_f32 v185, v162, v163\n\t"
        "v_cvt_pk_bf16_f32 v186, v164, v165\n\t"
        "v_cvt_pk_bf16_f32 v187, v166, v167\n\t"
        "s_nop 1\n\t"
        "v_permlane16_swap_b32 v184, v186\n\t"
        "v_permlane16_swap_b32 v185, v187\n\t"
        "buffer_store_dwordx4 v[184:187], v244, s[48:51], s75 offen offset:128 sc1\n\t"
        "v_accvgpr_read_b32 v168, a108\n\t"
        "v_accvgpr_read_b32 v169, a109\n\t"
        "v_accvgpr_read_b32 v170, a110\n\t"
        "v_accvgpr_read_b32 v171, a111\n\t"
        "v_accvgpr_read_b32 v172, a124\n\t"
        "v_accvgpr_read_b32 v173, a125\n\t"
        "v_accvgpr_read_b32 v174, a126\n\t"
        "v_accvgpr_read_b32 v175, a127\n\t"
        "v_pk_mul_f32 v[168:169], v[168:169], v[142:143]\n\t"
        "v_pk_mul_f32 v[170:171], v[170:171], v[142:143]\n\t"
        "v_pk_mul_f32 v[172:173], v[172:173], v[142:143]\n\t"
        "v_pk_mul_f32 v[174:175], v[174:175], v[142:143]\n\t"
        "s_waitcnt lgkmcnt(0)\n\t"
        "v_pk_fma_f32 v[168:169], v[120:121], v[150:151], v[168:169]\n\t"
        "v_pk_fma_f32 v[170:171], v[122:123], v[150:151], v[170:171]\n\t"
        "v_pk_fma_f32 v[172:173], v[124:125], v[150:151], v[172:173]\n\t"
        "v_pk_fma_f32 v[174:175], v[126:127], v[150:151], v[174:175]\n\t"
        "v_cvt_pk_bf16_f32 v188, v168, v169\n\t"
        "v_cvt_pk_bf16_f32 v189, v170, v171\n\t"
        "v_cvt_pk_bf16_f32 v190, v172, v173\n\t"
        "v_cvt_pk_bf16_f32 v191, v174, v175\n\t"
        "s_nop 1\n\t"
        "v_permlane16_swap_b32 v188, v190\n\t"
        "v_permlane16_swap_b32 v189, v191\n\t"
        "buffer_store_dwordx4 v[188:191], v244, s[48:51], s75 offen offset:192 sc1\n\t"
        "s_branch DONE_%=\n\t"
        "SLOWALL_%=:\n\t"
        "s_mov_b32 s67, 1\n\t"
        "DONE_%=:\n\t"
        "s_waitcnt vmcnt(0) lgkmcnt(0)\n\t"
        "s_mov_b32 %[flag], s67\n\t"
        "s_branch END_%=\n\t"
        "SPEC0_%=:\n\t"
        "s_cmp_ge_u32 s58, s57\n\t"
        "s_cbranch_scc1 TAIL0_%=\n\t"
        "s_mov_b64 s[48:49], %[o]\n\t"
        "s_add_u32 s48, s48, s92\n\t"
        "s_addc_u32 s49, s49, 0\n\t"
        "s_and_b32 s49, s49, 0xffff\n\t"
        "buffer_load_dword v249, off, s[48:51], 0\n\t"
        "s_lshl_b32 s71, s58, 6\n\t"
        "s_sub_u32 s71, s59, s71\n\t"
        "s_lshl_b32 s88, s68, 5\n\t"
        "s_sub_u32 s71, s71, s88\n\t"
        "v_lshrrev_b32 v160, 4, v238\n\t"
        "v_lshlrev_b32 v160, 2, v160\n\t"
        "v_sub_u32 v160, s71, v160\n\t"
        "v_mov_b32 v161, 0xd06e6b28\n\t"
        "v_mov_b32 v162, 0\n\t"
        "v_cmp_lt_i32 vcc, 0, v160\n\t"
        "s_nop 1\n\t"
        "v_cndmask_b32 v240, v161, v162, vcc\n\t"
        "v_cmp_lt_i32 vcc, 1, v160\n\t"
        "s_nop 1\n\t"
        "v_cndmask_b32 v241, v161, v162, vcc\n\t"
        "v_cmp_lt_i32 vcc, 2, v160\n\t"
        "s_nop 1\n\t"
        "v_cndmask_b32 v242, v161, v162, vcc\n\t"
        "v_cmp_lt_i32 vcc, 3, v160\n\t"
        "s_nop 1\n\t"
        "v_cndmask_b32 v243, v161, v162, vcc\n\t"
        "v_cmp_lt_i32 vcc, 16, v160\n\t"
        "s_nop 1\n\t"
        "v_cndmask_b32 v244, v161, v162, vcc\n\t"
        "v_cmp_lt_i32 vcc, 17, v160\n\t"
        "s_nop 1\n\t"
        "v_cndmask_b32 v245, v161, v162, vcc\n\t"
        "v_cmp_lt_i32 vcc, 18, v160\n\t"
        "s_nop 1\n\t"
        "v_cndmask_b32 v246, v161, v162, vcc\n\t"
        "v_cmp_lt_i32 vcc, 19, v160\n\t"
        "s_nop 1\n\t"
        "v_cndmask_b32 v247, v161, v162, vcc\n\t"
        "s_nop 1\n\t"
        "s_branch SRET0_%=\n\t"
        "SPEC1_%=:\n\t"
        "s_cmp_ge_u32 s58, s57\n\t"
        "s_cbranch_scc1 TAIL1_%=\n\t"
        "s_mov_b64 s[48:49], %[o]\n\t"
        "s_add_u32 s48, s48, s92\n\t"
        "s_addc_u32 s49, s49, 0\n\t"
        "s_and_b32 s49, s49, 0xffff\n\t"
        "buffer_load_dword v249, off, s[48:51], 0\n\t"
        "s_lshl_b32 s71, s58, 6\n\t"
        "s_sub_u32 s71, s59, s71\n\t"
        "s_lshl_b32 s88, s68, 5\n\t"
        "s_sub_u32 s71, s71, s88\n\t"
        "v_lshrrev_b32 v128, 4, v238\n\t"
        "v_lshlrev_b32 v128, 2, v128\n\t"
        "v_sub_u32 v128, s71, v128\n\t"
        "v_mov_b32 v129, 0xd06e6b28\n\t"
        "v_mov_b32 v130, 0\n\t"
        "v_cmp_lt_i32 vcc, 0, v128\n\t"
        "s_nop 1\n\t"
        "v_cndmask_b32 v240, v129, v130, vcc\n\t"
        "v_cmp_lt_i32 vcc, 1, v128\n\t"
        "s_nop 1\n\t"
        "v_cndmask_b32 v241, v129, v130, vcc\n\t"
        "v_cmp_lt_i32 vcc, 2, v128\n\t"
        "s_nop 1\n\t"
        "v_cndmask_b32 v242, v129, v130, vcc\n\t"
        "v_cmp_lt_i32 vcc, 3, v128\n\t"
        "s_nop 1\n\t"
        "v_cndmask_b32 v243, v129, v130, vcc\n\t"
        "v_cmp_lt_i32 vcc, 16, v128\n\t"
        "s_nop 1\n\t"
        "v_cndmask_b32 v244, v129, v130, vcc\n\t"
        "v_cmp_lt_i32 vcc, 17, v128\n\t"
        "s_nop 1\n\t"
        "v_cndmask_b32 v245, v129, v130, vcc\n\t"
        "v_cmp_lt_i32 vcc, 18, v128\n\t"
        "s_nop 1\n\t"
        "v_cndmask_b32 v246, v129, v130, vcc\n\t"
        "v_cmp_lt_i32 vcc, 19, v128\n\t"
        "s_nop 1\n\t"
        "v_cndmask_b32 v247, v129, v130, vcc\n\t"
        "s_nop 1\n\t"
        "s_branch SRET1_%=\n\t"
        "RARE0_%=:\n\t"
        "s_nop 7\n\t"
        "s_nop 7\n\t"
        "v_mov_b32 v160, v224\n\t"
        "v_mov_b32 v161, v224\n\t"
        "s_nop 1\n\t"
        "v_permlane16_swap_b32 v160, v161\n\t"
        "s_nop 1\n\t"
        "v_max_f32 v160, v160, v161\n\t"
        "s_nop 0\n\t"
        "v_mov_b32 v161, v160\n\t"
        "s_nop 1\n\t"
        "v_permlane32_swap_b32 v160, v161\n\t"
        "s_nop 1\n\t"
        "v_max_f32 v160, v160, v161\n\t"
        "v_mov_b32 v161, 0x7f000000\n\t"
        "v_cmp_nlt_f32 vcc, v160, v161\n\t"
        "s_nop 4\n\t"
        "s_cmp_lg_u64 vcc, 0\n\t"
        "s_cselect_b32 s71, 1, 0\n\t"
        "s_or_b32 s67, s67, s71\n\t"
        "v_frexp_exp_i32_f32 v162, v160\n\t"
        "v_max_i32 v162, 0, v162\n\t"
        "v_sub_u32 v161, 0, v162\n\t"
        "v_ldexp_f32 v163, 1.0, v161\n\t"
        "v_cvt_f32_i32 v162, v162\n\t"
        "v_sub_f32 v225, v225, v162\n\t"
        "v_mul_f32 v224, v224, v163\n\t"
        "v_mov_b32 v161, 0x4b000000\n\t"
        "v_cmp_nlt_f32 vcc, |v225|, v161\n\t"
        "s_nop 4\n\t"
        "s_cmp_lg_u64 vcc, 0\n\t"
        "s_cselect_b32 s71, 1, 0\n\t"
        "s_or_b32 s67, s67, s71\n\t"
        "v_lshlrev_b32 v160, 16, v192\n\t"
        "v_and_b32 v161, 0xffff0000, v192\n\t"
        "v_mul_f32 v160, v160, v163\n\t"
        "v_mul_f32 v161, v161, v163\n\t"
        "v_cvt_pk_bf16_f32 v192, v160, v161\n\t"
        "v_lshlrev_b32 v160, 16, v193\n\t"
        "v_and_b32 v161, 0xffff0000, v193\n\t"
        "v_mul_f32 v160, v160, v163\n\t"
        "v_mul_f32 v161, v161, v163\n\t"
        "v_cvt_pk_bf16_f32 v193, v160, v161\n\t"
        "v_lshlrev_b32 v160, 16, v194\n\t"
        "v_and_b32 v161, 0xffff0000, v194\n\t"
        "v_mul_f32 v160, v160, v163\n\t"
        "v_mul_f32 v161, v161, v163\n\t"
        "v_cvt_pk_bf16_f32 v194, v160, v161\n\t"
        "v_lshlrev_b32 v160, 16, v195\n\t"
        "v_and_b32 v161, 0xffff0000, v195\n\t"
        "v_mul_f32 v160, v160, v163\n\t"
        "v_mul_f32 v161, v161, v163\n\t"
        "v_cvt_pk_bf16_f32 v195, v160, v161\n\t"
        "v_mov_b32 v160, v226\n\t"
        "v_mov_b32 v161, v226\n\t"
        "s_nop 1\n\t"
        "v_permlane16_swap_b32 v160, v161\n\t"
        "s_nop 1\n\t"
        "v_max_f32 v160, v160, v161\n\t"
        "s_nop 0\n\t"
        "v_mov_b32 v161, v160\n\t"
        "s_nop 1\n\t"
        "v_permlane32_swap_b32 v160, v161\n\t"
        "s_nop 1\n\t"
        "v_max_f32 v160, v160, v161\n\t"
        "v_mov_b32 v161, 0x7f000000\n\t"
        "v_cmp_nlt_f32 vcc, v160, v161\n\t"
        "s_nop 4\n\t"
        "s_cmp_lg_u64 vcc, 0\n\t"
        "s_cselect_b32 s71, 1, 0\n\t"
        "s_or_b32 s67, s67, s71\n\t"
        "v_frexp_exp_i32_f32 v162, v160\n\t"
        "v_max_i32 v162, 0, v162\n\t"
        "v_sub_u32 v161, 0, v162\n\t"
        "v_ldexp_f32 v164, 1.0, v161\n\t"
        "v_cvt_f32_i32 v162, v162\n\t"
        "v_sub_f32 v227, v227, v162\n\t"
        "v_mul_f32 v226, v226, v164\n\t"
        "v_mov_b32 v161, 0x4b000000\n\t"
        "v_cmp_nlt_f32 vcc, |v227|, v161\n\t"
        "s_nop 4\n\t"
        "s_cmp_lg_u64 vcc, 0\n\t"
        "s_cselect_b32 s71, 1, 0\n\t"
        "s_or_b32 s67, s67, s71\n\t"
        "v_lshlrev_b32 v160, 16, v196\n\t"
        "v_and_b32 v161, 0xffff0000, v196\n\t"
        "v_mul_f32 v160, v160, v164\n\t"
        "v_mul_f32 v161, v161, v164\n\t"
        "v_cvt_pk_bf16_f32 v196, v160, v161\n\t"
        "v_lshlrev_b32 v160, 16, v197\n\t"
        "v_and_b32 v161, 0xffff0000, v197\n\t"
        "v_mul_f32 v160, v160, v164\n\t"
        "v_mul_f32 v161, v161, v164\n\t"
        "v_cvt_pk_bf16_f32 v197, v160, v161\n\t"
        "v_lshlrev_b32 v160, 16, v198\n\t"
        "v_and_b32 v161, 0xffff0000, v198\n\t"
        "v_mul_f32 v160, v160, v164\n\t"
        "v_mul_f32 v161, v161, v164\n\t"
        "v_cvt_pk_bf16_f32 v198, v160, v161\n\t"
        "v_lshlrev_b32 v160, 16, v199\n\t"
        "v_and_b32 v161, 0xffff0000, v199\n\t"
        "v_mul_f32 v160, v160, v164\n\t"
        "v_mul_f32 v161, v161, v164\n\t"
        "v_cvt_pk_bf16_f32 v199, v160, v161\n\t"
        "v_mov_b32 v160, v228\n\t"
        "v_mov_b32 v161, v228\n\t"
        "s_nop 1\n\t"
        "v_permlane16_swap_b32 v160, v161\n\t"
        "s_nop 1\n\t"
        "v_max_f32 v160, v160, v161\n\t"
        "s_nop 0\n\t"
        "v_mov_b32 v161, v160\n\t"
        "s_nop 1\n\t"
        "v_permlane32_swap_b32 v160, v161\n\t"
        "s_nop 1\n\t"
        "v_max_f32 v160, v160, v161\n\t"
        "v_mov_b32 v161, 0x7f000000\n\t"
        "v_cmp_nlt_f32 vcc, v160, v161\n\t"
        "s_nop 4\n\t"
        "s_cmp_lg_u64 vcc, 0\n\t"
        "s_cselect_b32 s71, 1, 0\n\t"
        "s_or_b32 s67, s67, s71\n\t"
        "v_frexp_exp_i32_f32 v162, v160\n\t"
        "v_max_i32 v162, 0, v162\n\t"
        "v_sub_u32 v161, 0, v162\n\t"
        "v_ldexp_f32 v165, 1.0, v161\n\t"
        "v_cvt_f32_i32 v162, v162\n\t"
        "v_sub_f32 v229, v229, v162\n\t"
        "v_mul_f32 v228, v228, v165\n\t"
        "v_mov_b32 v161, 0x4b000000\n\t"
        "v_cmp_nlt_f32 vcc, |v229|, v161\n\t"
        "s_nop 4\n\t"
        "s_cmp_lg_u64 vcc, 0\n\t"
        "s_cselect_b32 s71, 1, 0\n\t"
        "s_or_b32 s67, s67, s71\n\t"
        "v_lshlrev_b32 v160, 16, v200\n\t"
        "v_and_b32 v161, 0xffff0000, v200\n\t"
        "v_mul_f32 v160, v160, v165\n\t"
        "v_mul_f32 v161, v161, v165\n\t"
        "v_cvt_pk_bf16_f32 v200, v160, v161\n\t"
        "v_lshlrev_b32 v160, 16, v201\n\t"
        "v_and_b32 v161, 0xffff0000, v201\n\t"
        "v_mul_f32 v160, v160, v165\n\t"
        "v_mul_f32 v161, v161, v165\n\t"
        "v_cvt_pk_bf16_f32 v201, v160, v161\n\t"
        "v_lshlrev_b32 v160, 16, v202\n\t"
        "v_and_b32 v161, 0xffff0000, v202\n\t"
        "v_mul_f32 v160, v160, v165\n\t"
        "v_mul_f32 v161, v161, v165\n\t"
        "v_cvt_pk_bf16_f32 v202, v160, v161\n\t"
        "v_lshlrev_b32 v160, 16, v203\n\t"
        "v_and_b32 v161, 0xffff0000, v203\n\t"
        "v_mul_f32 v160, v160, v165\n\t"
        "v_mul_f32 v161, v161, v165\n\t"
        "v_cvt_pk_bf16_f32 v203, v160, v161\n\t"
        "v_mov_b32 v160, v230\n\t"
        "v_mov_b32 v161, v230\n\t"
        "s_nop 1\n\t"
        "v_permlane16_swap_b32 v160, v161\n\t"
        "s_nop 1\n\t"
        "v_max_f32 v160, v160, v161\n\t"
        "s_nop 0\n\t"
        "v_mov_b32 v161, v160\n\t"
        "s_nop 1\n\t"
        "v_permlane32_swap_b32 v160, v161\n\t"
        "s_nop 1\n\t"
        "v_max_f32 v160, v160, v161\n\t"
        "v_mov_b32 v161, 0x7f000000\n\t"
        "v_cmp_nlt_f32 vcc, v160, v161\n\t"
        "s_nop 4\n\t"
        "s_cmp_lg_u64 vcc, 0\n\t"
        "s_cselect_b32 s71, 1, 0\n\t"
        "s_or_b32 s67, s67, s71\n\t"
        "v_frexp_exp_i32_f32 v162, v160\n\t"
        "v_max_i32 v162, 0, v162\n\t"
        "v_sub_u32 v161, 0, v162\n\t"
        "v_ldexp_f32 v166, 1.0, v161\n\t"
        "v_cvt_f32_i32 v162, v162\n\t"
        "v_sub_f32 v231, v231, v162\n\t"
        "v_mul_f32 v230, v230, v166\n\t"
        "v_mov_b32 v161, 0x4b000000\n\t"
        "v_cmp_nlt_f32 vcc, |v231|, v161\n\t"
        "s_nop 4\n\t"
        "s_cmp_lg_u64 vcc, 0\n\t"
        "s_cselect_b32 s71, 1, 0\n\t"
        "s_or_b32 s67, s67, s71\n\t"
        "v_lshlrev_b32 v160, 16, v204\n\t"
        "v_and_b32 v161, 0xffff0000, v204\n\t"
        "v_mul_f32 v160, v160, v166\n\t"
        "v_mul_f32 v161, v161, v166\n\t"
        "v_cvt_pk_bf16_f32 v204, v160, v161\n\t"
        "v_lshlrev_b32 v160, 16, v205\n\t"
        "v_and_b32 v161, 0xffff0000, v205\n\t"
        "v_mul_f32 v160, v160, v166\n\t"
        "v_mul_f32 v161, v161, v166\n\t"
        "v_cvt_pk_bf16_f32 v205, v160, v161\n\t"
        "v_lshlrev_b32 v160, 16, v206\n\t"
        "v_and_b32 v161, 0xffff0000, v206\n\t"
        "v_mul_f32 v160, v160, v166\n\t"
        "v_mul_f32 v161, v161, v166\n\t"
        "v_cvt_pk_bf16_f32 v206, v160, v161\n\t"
        "v_lshlrev_b32 v160, 16, v207\n\t"
        "v_and_b32 v161, 0xffff0000, v207\n\t"
        "v_mul_f32 v160, v160, v166\n\t"
        "v_mul_f32 v161, v161, v166\n\t"
        "v_cvt_pk_bf16_f32 v207, v160, v161\n\t"
        "v_accvgpr_read_b32 v160, a0\n\t"
        "v_accvgpr_read_b32 v161, a1\n\t"
        "v_accvgpr_read_b32 v162, a2\n\t"
        "v_accvgpr_read_b32 v167, a3\n\t"
        "v_mul_f32 v160, v160, v163\n\t"
        "v_mul_f32 v161, v161, v163\n\t"
        "v_mul_f32 v162, v162, v163\n\t"
        "v_mul_f32 v167, v167, v163\n\t"
        "v_accvgpr_write_b32 a0, v160\n\t"
        "v_accvgpr_write_b32 a1, v161\n\t"
        "v_accvgpr_write_b32 a2, v162\n\t"
        "v_accvgpr_write_b32 a3, v167\n\t"
        "v_accvgpr_read_b32 v160, a4\n\t"
        "v_accvgpr_read_b32 v161, a5\n\t"
        "v_accvgpr_read_b32 v162, a6\n\t"
        "v_accvgpr_read_b32 v167, a7\n\t"
        "v_mul_f32 v160, v160, v164\n\t"
        "v_mul_f32 v161, v161, v164\n\t"
        "v_mul_f32 v162, v162, v164\n\t"
        "v_mul_f32 v167, v167, v164\n\t"
        "v_accvgpr_write_b32 a4, v160\n\t"
        "v_accvgpr_write_b32 a5, v161\n\t"
        "v_accvgpr_write_b32 a6, v162\n\t"
        "v_accvgpr_write_b32 a7, v167\n\t"
        "v_accvgpr_read_b32 v160, a8\n\t"
        "v_accvgpr_read_b32 v161, a9\n\t"
        "v_accvgpr_read_b32 v162, a10\n\t"
        "v_accvgpr_read_b32 v167, a11\n\t"
        "v_mul_f32 v160, v160, v165\n\t"
        "v_mul_f32 v161, v161, v165\n\t"
        "v_mul_f32 v162, v162, v165\n\t"
        "v_mul_f32 v167, v167, v165\n\t"
        "v_accvgpr_write_b32 a8, v160\n\t"
        "v_accvgpr_write_b32 a9, v161\n\t"
        "v_accvgpr_write_b32 a10, v162\n\t"
        "v_accvgpr_write_b32 a11, v167\n\t"
        "v_accvgpr_read_b32 v160, a12\n\t"
        "v_accvgpr_read_b32 v161, a13\n\t"
        "v_accvgpr_read_b32 v162, a14\n\t"
        "v_accvgpr_read_b32 v167, a15\n\t"
        "v_mul_f32 v160, v160, v166\n\t"
        "v_mul_f32 v161, v161, v166\n\t"
        "v_mul_f32 v162, v162, v166\n\t"
        "v_mul_f32 v167, v167, v166\n\t"
        "v_accvgpr_write_b32 a12, v160\n\t"
        "v_accvgpr_write_b32 a13, v161\n\t"
        "v_accvgpr_write_b32 a14, v162\n\t"
        "v_accvgpr_write_b32 a15, v167\n\t"
        "v_accvgpr_read_b32 v160, a16\n\t"
        "v_accvgpr_read_b32 v161, a17\n\t"
        "v_accvgpr_read_b32 v162, a18\n\t"
        "v_accvgpr_read_b32 v167, a19\n\t"
        "v_mul_f32 v160, v160, v163\n\t"
        "v_mul_f32 v161, v161, v163\n\t"
        "v_mul_f32 v162, v162, v163\n\t"
        "v_mul_f32 v167, v167, v163\n\t"
        "v_accvgpr_write_b32 a16, v160\n\t"
        "v_accvgpr_write_b32 a17, v161\n\t"
        "v_accvgpr_write_b32 a18, v162\n\t"
        "v_accvgpr_write_b32 a19, v167\n\t"
        "v_accvgpr_read_b32 v160, a20\n\t"
        "v_accvgpr_read_b32 v161, a21\n\t"
        "v_accvgpr_read_b32 v162, a22\n\t"
        "v_accvgpr_read_b32 v167, a23\n\t"
        "v_mul_f32 v160, v160, v164\n\t"
        "v_mul_f32 v161, v161, v164\n\t"
        "v_mul_f32 v162, v162, v164\n\t"
        "v_mul_f32 v167, v167, v164\n\t"
        "v_accvgpr_write_b32 a20, v160\n\t"
        "v_accvgpr_write_b32 a21, v161\n\t"
        "v_accvgpr_write_b32 a22, v162\n\t"
        "v_accvgpr_write_b32 a23, v167\n\t"
        "v_accvgpr_read_b32 v160, a24\n\t"
        "v_accvgpr_read_b32 v161, a25\n\t"
        "v_accvgpr_read_b32 v162, a26\n\t"
        "v_accvgpr_read_b32 v167, a27\n\t"
        "v_mul_f32 v160, v160, v165\n\t"
        "v_mul_f32 v161, v161, v165\n\t"
        "v_mul_f32 v162, v162, v165\n\t"
        "v_mul_f32 v167, v167, v165\n\t"
        "v_accvgpr_write_b32 a24, v160\n\t"
        "v_accvgpr_write_b32 a25, v161\n\t"
        "v_accvgpr_write_b32 a26, v162\n\t"
        "v_accvgpr_write_b32 a27, v167\n\t"
        "v_accvgpr_read_b32 v160, a28\n\t"
        "v_accvgpr_read_b32 v161, a29\n\t"
        "v_accvgpr_read_b32 v162, a30\n\t"
        "v_accvgpr_read_b32 v167, a31\n\t"
        "v_mul_f32 v160, v160, v166\n\t"
        "v_mul_f32 v161, v161, v166\n\t"
        "v_mul_f32 v162, v162, v166\n\t"
        "v_mul_f32 v167, v167, v166\n\t"
        "v_accvgpr_write_b32 a28, v160\n\t"
        "v_accvgpr_write_b32 a29, v161\n\t"
        "v_accvgpr_write_b32 a30, v162\n\t"
        "v_accvgpr_write_b32 a31, v167\n\t"
        "v_accvgpr_read_b32 v160, a32\n\t"
        "v_accvgpr_read_b32 v161, a33\n\t"
        "v_accvgpr_read_b32 v162, a34\n\t"
        "v_accvgpr_read_b32 v167, a35\n\t"
        "v_mul_f32 v160, v160, v163\n\t"
        "v_mul_f32 v161, v161, v163\n\t"
        "v_mul_f32 v162, v162, v163\n\t"
        "v_mul_f32 v167, v167, v163\n\t"
        "v_accvgpr_write_b32 a32, v160\n\t"
        "v_accvgpr_write_b32 a33, v161\n\t"
        "v_accvgpr_write_b32 a34, v162\n\t"
        "v_accvgpr_write_b32 a35, v167\n\t"
        "v_accvgpr_read_b32 v160, a36\n\t"
        "v_accvgpr_read_b32 v161, a37\n\t"
        "v_accvgpr_read_b32 v162, a38\n\t"
        "v_accvgpr_read_b32 v167, a39\n\t"
        "v_mul_f32 v160, v160, v164\n\t"
        "v_mul_f32 v161, v161, v164\n\t"
        "v_mul_f32 v162, v162, v164\n\t"
        "v_mul_f32 v167, v167, v164\n\t"
        "v_accvgpr_write_b32 a36, v160\n\t"
        "v_accvgpr_write_b32 a37, v161\n\t"
        "v_accvgpr_write_b32 a38, v162\n\t"
        "v_accvgpr_write_b32 a39, v167\n\t"
        "v_accvgpr_read_b32 v160, a40\n\t"
        "v_accvgpr_read_b32 v161, a41\n\t"
        "v_accvgpr_read_b32 v162, a42\n\t"
        "v_accvgpr_read_b32 v167, a43\n\t"
        "v_mul_f32 v160, v160, v165\n\t"
        "v_mul_f32 v161, v161, v165\n\t"
        "v_mul_f32 v162, v162, v165\n\t"
        "v_mul_f32 v167, v167, v165\n\t"
        "v_accvgpr_write_b32 a40, v160\n\t"
        "v_accvgpr_write_b32 a41, v161\n\t"
        "v_accvgpr_write_b32 a42, v162\n\t"
        "v_accvgpr_write_b32 a43, v167\n\t"
        "v_accvgpr_read_b32 v160, a44\n\t"
        "v_accvgpr_read_b32 v161, a45\n\t"
        "v_accvgpr_read_b32 v162, a46\n\t"
        "v_accvgpr_read_b32 v167, a47\n\t"
        "v_mul_f32 v160, v160, v166\n\t"
        "v_mul_f32 v161, v161, v166\n\t"
        "v_mul_f32 v162, v162, v166\n\t"
        "v_mul_f32 v167, v167, v166\n\t"
        "v_accvgpr_write_b32 a44, v160\n\t"
        "v_accvgpr_write_b32 a45, v161\n\t"
        "v_accvgpr_write_b32 a46, v162\n\t"
        "v_accvgpr_write_b32 a47, v167\n\t"
        "v_accvgpr_read_b32 v160, a48\n\t"
        "v_accvgpr_read_b32 v161, a49\n\t"
        "v_accvgpr_read_b32 v162, a50\n\t"
        "v_accvgpr_read_b32 v167, a51\n\t"
        "v_mul_f32 v160, v160, v163\n\t"
        "v_mul_f32 v161, v161, v163\n\t"
        "v_mul_f32 v162, v162, v163\n\t"
        "v_mul_f32 v167, v167, v163\n\t"
        "v_accvgpr_write_b32 a48, v160\n\t"
        "v_accvgpr_write_b32 a49, v161\n\t"
        "v_accvgpr_write_b32 a50, v162\n\t"
        "v_accvgpr_write_b32 a51, v167\n\t"
        "v_accvgpr_read_b32 v160, a52\n\t"
        "v_accvgpr_read_b32 v161, a53\n\t"
        "v_accvgpr_read_b32 v162, a54\n\t"
        "v_accvgpr_read_b32 v167, a55\n\t"
        "v_mul_f32 v160, v160, v164\n\t"
        "v_mul_f32 v161, v161, v164\n\t"
        "v_mul_f32 v162, v162, v164\n\t"
        "v_mul_f32 v167, v167, v164\n\t"
        "v_accvgpr_write_b32 a52, v160\n\t"
        "v_accvgpr_write_b32 a53, v161\n\t"
        "v_accvgpr_write_b32 a54, v162\n\t"
        "v_accvgpr_write_b32 a55, v167\n\t"
        "v_accvgpr_read_b32 v160, a56\n\t"
        "v_accvgpr_read_b32 v161, a57\n\t"
        "v_accvgpr_read_b32 v162, a58\n\t"
        "v_accvgpr_read_b32 v167, a59\n\t"
        "v_mul_f32 v160, v160, v165\n\t"
        "v_mul_f32 v161, v161, v165\n\t"
        "v_mul_f32 v162, v162, v165\n\t"
        "v_mul_f32 v167, v167, v165\n\t"
        "v_accvgpr_write_b32 a56, v160\n\t"
        "v_accvgpr_write_b32 a57, v161\n\t"
        "v_accvgpr_write_b32 a58, v162\n\t"
        "v_accvgpr_write_b32 a59, v167\n\t"
        "v_accvgpr_read_b32 v160, a60\n\t"
        "v_accvgpr_read_b32 v161, a61\n\t"
        "v_accvgpr_read_b32 v162, a62\n\t"
        "v_accvgpr_read_b32 v167, a63\n\t"
        "v_mul_f32 v160, v160, v166\n\t"
        "v_mul_f32 v161, v161, v166\n\t"
        "v_mul_f32 v162, v162, v166\n\t"
        "v_mul_f32 v167, v167, v166\n\t"
        "v_accvgpr_write_b32 a60, v160\n\t"
        "v_accvgpr_write_b32 a61, v161\n\t"
        "v_accvgpr_write_b32 a62, v162\n\t"
        "v_accvgpr_write_b32 a63, v167\n\t"
        "v_accvgpr_read_b32 v160, a64\n\t"
        "v_accvgpr_read_b32 v161, a65\n\t"
        "v_accvgpr_read_b32 v162, a66\n\t"
        "v_accvgpr_read_b32 v167, a67\n\t"
        "v_mul_f32 v160, v160, v163\n\t"
        "v_mul_f32 v161, v161, v163\n\t"
        "v_mul_f32 v162, v162, v163\n\t"
        "v_mul_f32 v167, v167, v163\n\t"
        "v_accvgpr_write_b32 a64, v160\n\t"
        "v_accvgpr_write_b32 a65, v161\n\t"
        "v_accvgpr_write_b32 a66, v162\n\t"
        "v_accvgpr_write_b32 a67, v167\n\t"
        "v_accvgpr_read_b32 v160, a68\n\t"
        "v_accvgpr_read_b32 v161, a69\n\t"
        "v_accvgpr_read_b32 v162, a70\n\t"
        "v_accvgpr_read_b32 v167, a71\n\t"
        "v_mul_f32 v160, v160, v164\n\t"
        "v_mul_f32 v161, v161, v164\n\t"
        "v_mul_f32 v162, v162, v164\n\t"
        "v_mul_f32 v167, v167, v164\n\t"
        "v_accvgpr_write_b32 a68, v160\n\t"
        "v_accvgpr_write_b32 a69, v161\n\t"
        "v_accvgpr_write_b32 a70, v162\n\t"
        "v_accvgpr_write_b32 a71, v167\n\t"
        "v_accvgpr_read_b32 v160, a72\n\t"
        "v_accvgpr_read_b32 v161, a73\n\t"
        "v_accvgpr_read_b32 v162, a74\n\t"
        "v_accvgpr_read_b32 v167, a75\n\t"
        "v_mul_f32 v160, v160, v165\n\t"
        "v_mul_f32 v161, v161, v165\n\t"
        "v_mul_f32 v162, v162, v165\n\t"
        "v_mul_f32 v167, v167, v165\n\t"
        "v_accvgpr_write_b32 a72, v160\n\t"
        "v_accvgpr_write_b32 a73, v161\n\t"
        "v_accvgpr_write_b32 a74, v162\n\t"
        "v_accvgpr_write_b32 a75, v167\n\t"
        "v_accvgpr_read_b32 v160, a76\n\t"
        "v_accvgpr_read_b32 v161, a77\n\t"
        "v_accvgpr_read_b32 v162, a78\n\t"
        "v_accvgpr_read_b32 v167, a79\n\t"
        "v_mul_f32 v160, v160, v166\n\t"
        "v_mul_f32 v161, v161, v166\n\t"
        "v_mul_f32 v162, v162, v166\n\t"
        "v_mul_f32 v167, v167, v166\n\t"
        "v_accvgpr_write_b32 a76, v160\n\t"
        "v_accvgpr_write_b32 a77, v161\n\t"
        "v_accvgpr_write_b32 a78, v162\n\t"
        "v_accvgpr_write_b32 a79, v167\n\t"
        "v_accvgpr_read_b32 v160, a80\n\t"
        "v_accvgpr_read_b32 v161, a81\n\t"
        "v_accvgpr_read_b32 v162, a82\n\t"
        "v_accvgpr_read_b32 v167, a83\n\t"
        "v_mul_f32 v160, v160, v163\n\t"
        "v_mul_f32 v161, v161, v163\n\t"
        "v_mul_f32 v162, v162, v163\n\t"
        "v_mul_f32 v167, v167, v163\n\t"
        "v_accvgpr_write_b32 a80, v160\n\t"
        "v_accvgpr_write_b32 a81, v161\n\t"
        "v_accvgpr_write_b32 a82, v162\n\t"
        "v_accvgpr_write_b32 a83, v167\n\t"
        "v_accvgpr_read_b32 v160, a84\n\t"
        "v_accvgpr_read_b32 v161, a85\n\t"
        "v_accvgpr_read_b32 v162, a86\n\t"
        "v_accvgpr_read_b32 v167, a87\n\t"
        "v_mul_f32 v160, v160, v164\n\t"
        "v_mul_f32 v161, v161, v164\n\t"
        "v_mul_f32 v162, v162, v164\n\t"
        "v_mul_f32 v167, v167, v164\n\t"
        "v_accvgpr_write_b32 a84, v160\n\t"
        "v_accvgpr_write_b32 a85, v161\n\t"
        "v_accvgpr_write_b32 a86, v162\n\t"
        "v_accvgpr_write_b32 a87, v167\n\t"
        "v_accvgpr_read_b32 v160, a88\n\t"
        "v_accvgpr_read_b32 v161, a89\n\t"
        "v_accvgpr_read_b32 v162, a90\n\t"
        "v_accvgpr_read_b32 v167, a91\n\t"
        "v_mul_f32 v160, v160, v165\n\t"
        "v_mul_f32 v161, v161, v165\n\t"
        "v_mul_f32 v162, v162, v165\n\t"
        "v_mul_f32 v167, v167, v165\n\t"
        "v_accvgpr_write_b32 a88, v160\n\t"
        "v_accvgpr_write_b32 a89, v161\n\t"
        "v_accvgpr_write_b32 a90, v162\n\t"
        "v_accvgpr_write_b32 a91, v167\n\t"
        "v_accvgpr_read_b32 v160, a92\n\t"
        "v_accvgpr_read_b32 v161, a93\n\t"
        "v_accvgpr_read_b32 v162, a94\n\t"
        "v_accvgpr_read_b32 v167, a95\n\t"
        "v_mul_f32 v160, v160, v166\n\t"
        "v_mul_f32 v161, v161, v166\n\t"
        "v_mul_f32 v162, v162, v166\n\t"
        "v_mul_f32 v167, v167, v166\n\t"
        "v_accvgpr_write_b32 a92, v160\n\t"
        "v_accvgpr_write_b32 a93, v161\n\t"
        "v_accvgpr_write_b32 a94, v162\n\t"
        "v_accvgpr_write_b32 a95, v167\n\t"
        "v_accvgpr_read_b32 v160, a96\n\t"
        "v_accvgpr_read_b32 v161, a97\n\t"
        "v_accvgpr_read_b32 v162, a98\n\t"
        "v_accvgpr_read_b32 v167, a99\n\t"
        "v_mul_f32 v160, v160, v163\n\t"
        "v_mul_f32 v161, v161, v163\n\t"
        "v_mul_f32 v162, v162, v163\n\t"
        "v_mul_f32 v167, v167, v163\n\t"
        "v_accvgpr_write_b32 a96, v160\n\t"
        "v_accvgpr_write_b32 a97, v161\n\t"
        "v_accvgpr_write_b32 a98, v162\n\t"
        "v_accvgpr_write_b32 a99, v167\n\t"
        "v_accvgpr_read_b32 v160, a100\n\t"
        "v_accvgpr_read_b32 v161, a101\n\t"
        "v_accvgpr_read_b32 v162, a102\n\t"
        "v_accvgpr_read_b32 v167, a103\n\t"
        "v_mul_f32 v160, v160, v164\n\t"
        "v_mul_f32 v161, v161, v164\n\t"
        "v_mul_f32 v162, v162, v164\n\t"
        "v_mul_f32 v167, v167, v164\n\t"
        "v_accvgpr_write_b32 a100, v160\n\t"
        "v_accvgpr_write_b32 a101, v161\n\t"
        "v_accvgpr_write_b32 a102, v162\n\t"
        "v_accvgpr_write_b32 a103, v167\n\t"
        "v_accvgpr_read_b32 v160, a104\n\t"
        "v_accvgpr_read_b32 v161, a105\n\t"
        "v_accvgpr_read_b32 v162, a106\n\t"
        "v_accvgpr_read_b32 v167, a107\n\t"
        "v_mul_f32 v160, v160, v165\n\t"
        "v_mul_f32 v161, v161, v165\n\t"
        "v_mul_f32 v162, v162, v165\n\t"
        "v_mul_f32 v167, v167, v165\n\t"
        "v_accvgpr_write_b32 a104, v160\n\t"
        "v_accvgpr_write_b32 a105, v161\n\t"
        "v_accvgpr_write_b32 a106, v162\n\t"
        "v_accvgpr_write_b32 a107, v167\n\t"
        "v_accvgpr_read_b32 v160, a108\n\t"
        "v_accvgpr_read_b32 v161, a109\n\t"
        "v_accvgpr_read_b32 v162, a110\n\t"
        "v_accvgpr_read_b32 v167, a111\n\t"
        "v_mul_f32 v160, v160, v166\n\t"
        "v_mul_f32 v161, v161, v166\n\t"
        "v_mul_f32 v162, v162, v166\n\t"
        "v_mul_f32 v167, v167, v166\n\t"
        "v_accvgpr_write_b32 a108, v160\n\t"
        "v_accvgpr_write_b32 a109, v161\n\t"
        "v_accvgpr_write_b32 a110, v162\n\t"
        "v_accvgpr_write_b32 a111, v167\n\t"
        "v_accvgpr_read_b32 v160, a112\n\t"
        "v_accvgpr_read_b32 v161, a113\n\t"
        "v_accvgpr_read_b32 v162, a114\n\t"
        "v_accvgpr_read_b32 v167, a115\n\t"
        "v_mul_f32 v160, v160, v163\n\t"
        "v_mul_f32 v161, v161, v163\n\t"
        "v_mul_f32 v162, v162, v163\n\t"
        "v_mul_f32 v167, v167, v163\n\t"
        "v_accvgpr_write_b32 a112, v160\n\t"
        "v_accvgpr_write_b32 a113, v161\n\t"
        "v_accvgpr_write_b32 a114, v162\n\t"
        "v_accvgpr_write_b32 a115, v167\n\t"
        "v_accvgpr_read_b32 v160, a116\n\t"
        "v_accvgpr_read_b32 v161, a117\n\t"
        "v_accvgpr_read_b32 v162, a118\n\t"
        "v_accvgpr_read_b32 v167, a119\n\t"
        "v_mul_f32 v160, v160, v164\n\t"
        "v_mul_f32 v161, v161, v164\n\t"
        "v_mul_f32 v162, v162, v164\n\t"
        "v_mul_f32 v167, v167, v164\n\t"
        "v_accvgpr_write_b32 a116, v160\n\t"
        "v_accvgpr_write_b32 a117, v161\n\t"
        "v_accvgpr_write_b32 a118, v162\n\t"
        "v_accvgpr_write_b32 a119, v167\n\t"
        "v_accvgpr_read_b32 v160, a120\n\t"
        "v_accvgpr_read_b32 v161, a121\n\t"
        "v_accvgpr_read_b32 v162, a122\n\t"
        "v_accvgpr_read_b32 v167, a123\n\t"
        "v_mul_f32 v160, v160, v165\n\t"
        "v_mul_f32 v161, v161, v165\n\t"
        "v_mul_f32 v162, v162, v165\n\t"
        "v_mul_f32 v167, v167, v165\n\t"
        "v_accvgpr_write_b32 a120, v160\n\t"
        "v_accvgpr_write_b32 a121, v161\n\t"
        "v_accvgpr_write_b32 a122, v162\n\t"
        "v_accvgpr_write_b32 a123, v167\n\t"
        "v_accvgpr_read_b32 v160, a124\n\t"
        "v_accvgpr_read_b32 v161, a125\n\t"
        "v_accvgpr_read_b32 v162, a126\n\t"
        "v_accvgpr_read_b32 v167, a127\n\t"
        "v_mul_f32 v160, v160, v166\n\t"
        "v_mul_f32 v161, v161, v166\n\t"
        "v_mul_f32 v162, v162, v166\n\t"
        "v_mul_f32 v167, v167, v166\n\t"
        "v_accvgpr_write_b32 a124, v160\n\t"
        "v_accvgpr_write_b32 a125, v161\n\t"
        "v_accvgpr_write_b32 a126, v162\n\t"
        "v_accvgpr_write_b32 a127, v167\n\t"
        "v_accvgpr_read_b32 v160, a128\n\t"
        "v_accvgpr_read_b32 v161, a129\n\t"
        "v_accvgpr_read_b32 v162, a130\n\t"
        "v_accvgpr_read_b32 v167, a131\n\t"
        "v_mul_f32 v160, v160, v163\n\t"
        "v_mul_f32 v161, v161, v163\n\t"
        "v_mul_f32 v162, v162, v163\n\t"
        "v_mul_f32 v167, v167, v163\n\t"
        "v_accvgpr_write_b32 a128, v160\n\t"
        "v_accvgpr_write_b32 a129, v161\n\t"
        "v_accvgpr_write_b32 a130, v162\n\t"
        "v_accvgpr_write_b32 a131, v167\n\t"
        "v_accvgpr_read_b32 v160, a132\n\t"
        "v_accvgpr_read_b32 v161, a133\n\t"
        "v_accvgpr_read_b32 v162, a134\n\t"
        "v_accvgpr_read_b32 v167, a135\n\t"
        "v_mul_f32 v160, v160, v164\n\t"
        "v_mul_f32 v161, v161, v164\n\t"
        "v_mul_f32 v162, v162, v164\n\t"
        "v_mul_f32 v167, v167, v164\n\t"
        "v_accvgpr_write_b32 a132, v160\n\t"
        "v_accvgpr_write_b32 a133, v161\n\t"
        "v_accvgpr_write_b32 a134, v162\n\t"
        "v_accvgpr_write_b32 a135, v167\n\t"
        "v_accvgpr_read_b32 v160, a136\n\t"
        "v_accvgpr_read_b32 v161, a137\n\t"
        "v_accvgpr_read_b32 v162, a138\n\t"
        "v_accvgpr_read_b32 v167, a139\n\t"
        "v_mul_f32 v160, v160, v165\n\t"
        "v_mul_f32 v161, v161, v165\n\t"
        "v_mul_f32 v162, v162, v165\n\t"
        "v_mul_f32 v167, v167, v165\n\t"
        "v_accvgpr_write_b32 a136, v160\n\t"
        "v_accvgpr_write_b32 a137, v161\n\t"
        "v_accvgpr_write_b32 a138, v162\n\t"
        "v_accvgpr_write_b32 a139, v167\n\t"
        "v_accvgpr_read_b32 v160, a140\n\t"
        "v_accvgpr_read_b32 v161, a141\n\t"
        "v_accvgpr_read_b32 v162, a142\n\t"
        "v_accvgpr_read_b32 v167, a143\n\t"
        "v_mul_f32 v160, v160, v166\n\t"
        "v_mul_f32 v161, v161, v166\n\t"
        "v_mul_f32 v162, v162, v166\n\t"
        "v_mul_f32 v167, v167, v166\n\t"
        "v_accvgpr_write_b32 a140, v160\n\t"
        "v_accvgpr_write_b32 a141, v161\n\t"
        "v_accvgpr_write_b32 a142, v162\n\t"
        "v_accvgpr_write_b32 a143, v167\n\t"
        "v_accvgpr_read_b32 v160, a144\n\t"
        "v_accvgpr_read_b32 v161, a145\n\t"
        "v_accvgpr_read_b32 v162, a146\n\t"
        "v_accvgpr_read_b32 v167, a147\n\t"
        "v_mul_f32 v160, v160, v163\n\t"
        "v_mul_f32 v161, v161, v163\n\t"
        "v_mul_f32 v162, v162, v163\n\t"
        "v_mul_f32 v167, v167, v163\n\t"
        "v_accvgpr_write_b32 a144, v160\n\t"
        "v_accvgpr_write_b32 a145, v161\n\t"
        "v_accvgpr_write_b32 a146, v162\n\t"
        "v_accvgpr_write_b32 a147, v167\n\t"
        "v_accvgpr_read_b32 v160, a148\n\t"
        "v_accvgpr_read_b32 v161, a149\n\t"
        "v_accvgpr_read_b32 v162, a150\n\t"
        "v_accvgpr_read_b32 v167, a151\n\t"
        "v_mul_f32 v160, v160, v164\n\t"
        "v_mul_f32 v161, v161, v164\n\t"
        "v_mul_f32 v162, v162, v164\n\t"
        "v_mul_f32 v167, v167, v164\n\t"
        "v_accvgpr_write_b32 a148, v160\n\t"
        "v_accvgpr_write_b32 a149, v161\n\t"
        "v_accvgpr_write_b32 a150, v162\n\t"
        "v_accvgpr_write_b32 a151, v167\n\t"
        "v_accvgpr_read_b32 v160, a152\n\t"
        "v_accvgpr_read_b32 v161, a153\n\t"
        "v_accvgpr_read_b32 v162, a154\n\t"
        "v_accvgpr_read_b32 v167, a155\n\t"
        "v_mul_f32 v160, v160, v165\n\t"
        "v_mul_f32 v161, v161, v165\n\t"
        "v_mul_f32 v162, v162, v165\n\t"
        "v_mul_f32 v167, v167, v165\n\t"
        "v_accvgpr_write_b32 a152, v160\n\t"
        "v_accvgpr_write_b32 a153, v161\n\t"
        "v_accvgpr_write_b32 a154, v162\n\t"
        "v_accvgpr_write_b32 a155, v167\n\t"
        "v_accvgpr_read_b32 v160, a156\n\t"
        "v_accvgpr_read_b32 v161, a157\n\t"
        "v_accvgpr_read_b32 v162, a158\n\t"
        "v_accvgpr_read_b32 v167, a159\n\t"
        "v_mul_f32 v160, v160, v166\n\t"
        "v_mul_f32 v161, v161, v166\n\t"
        "v_mul_f32 v162, v162, v166\n\t"
        "v_mul_f32 v167, v167, v166\n\t"
        "v_accvgpr_write_b32 a156, v160\n\t"
        "v_accvgpr_write_b32 a157, v161\n\t"
        "v_accvgpr_write_b32 a158, v162\n\t"
        "v_accvgpr_write_b32 a159, v167\n\t"
        "v_accvgpr_read_b32 v160, a160\n\t"
        "v_accvgpr_read_b32 v161, a161\n\t"
        "v_accvgpr_read_b32 v162, a162\n\t"
        "v_accvgpr_read_b32 v167, a163\n\t"
        "v_mul_f32 v160, v160, v163\n\t"
        "v_mul_f32 v161, v161, v163\n\t"
        "v_mul_f32 v162, v162, v163\n\t"
        "v_mul_f32 v167, v167, v163\n\t"
        "v_accvgpr_write_b32 a160, v160\n\t"
        "v_accvgpr_write_b32 a161, v161\n\t"
        "v_accvgpr_write_b32 a162, v162\n\t"
        "v_accvgpr_write_b32 a163, v167\n\t"
        "v_accvgpr_read_b32 v160, a164\n\t"
        "v_accvgpr_read_b32 v161, a165\n\t"
        "v_accvgpr_read_b32 v162, a166\n\t"
        "v_accvgpr_read_b32 v167, a167\n\t"
        "v_mul_f32 v160, v160, v164\n\t"
        "v_mul_f32 v161, v161, v164\n\t"
        "v_mul_f32 v162, v162, v164\n\t"
        "v_mul_f32 v167, v167, v164\n\t"
        "v_accvgpr_write_b32 a164, v160\n\t"
        "v_accvgpr_write_b32 a165, v161\n\t"
        "v_accvgpr_write_b32 a166, v162\n\t"
        "v_accvgpr_write_b32 a167, v167\n\t"
        "v_accvgpr_read_b32 v160, a168\n\t"
        "v_accvgpr_read_b32 v161, a169\n\t"
        "v_accvgpr_read_b32 v162, a170\n\t"
        "v_accvgpr_read_b32 v167, a171\n\t"
        "v_mul_f32 v160, v160, v165\n\t"
        "v_mul_f32 v161, v161, v165\n\t"
        "v_mul_f32 v162, v162, v165\n\t"
        "v_mul_f32 v167, v167, v165\n\t"
        "v_accvgpr_write_b32 a168, v160\n\t"
        "v_accvgpr_write_b32 a169, v161\n\t"
        "v_accvgpr_write_b32 a170, v162\n\t"
        "v_accvgpr_write_b32 a171, v167\n\t"
        "v_accvgpr_read_b32 v160, a172\n\t"
        "v_accvgpr_read_b32 v161, a173\n\t"
        "v_accvgpr_read_b32 v162, a174\n\t"
        "v_accvgpr_read_b32 v167, a175\n\t"
        "v_mul_f32 v160, v160, v166\n\t"
        "v_mul_f32 v161, v161, v166\n\t"
        "v_mul_f32 v162, v162, v166\n\t"
        "v_mul_f32 v167, v167, v166\n\t"
        "v_accvgpr_write_b32 a172, v160\n\t"
        "v_accvgpr_write_b32 a173, v161\n\t"
        "v_accvgpr_write_b32 a174, v162\n\t"
        "v_accvgpr_write_b32 a175, v167\n\t"
        "v_accvgpr_read_b32 v160, a176\n\t"
        "v_accvgpr_read_b32 v161, a177\n\t"
        "v_accvgpr_read_b32 v162, a178\n\t"
        "v_accvgpr_read_b32 v167, a179\n\t"
        "v_mul_f32 v160, v160, v163\n\t"
        "v_mul_f32 v161, v161, v163\n\t"
        "v_mul_f32 v162, v162, v163\n\t"
        "v_mul_f32 v167, v167, v163\n\t"
        "v_accvgpr_write_b32 a176, v160\n\t"
        "v_accvgpr_write_b32 a177, v161\n\t"
        "v_accvgpr_write_b32 a178, v162\n\t"
        "v_accvgpr_write_b32 a179, v167\n\t"
        "v_accvgpr_read_b32 v160, a180\n\t"
        "v_accvgpr_read_b32 v161, a181\n\t"
        "v_accvgpr_read_b32 v162, a182\n\t"
        "v_accvgpr_read_b32 v167, a183\n\t"
        "v_mul_f32 v160, v160, v164\n\t"
        "v_mul_f32 v161, v161, v164\n\t"
        "v_mul_f32 v162, v162, v164\n\t"
        "v_mul_f32 v167, v167, v164\n\t"
        "v_accvgpr_write_b32 a180, v160\n\t"
        "v_accvgpr_write_b32 a181, v161\n\t"
        "v_accvgpr_write_b32 a182, v162\n\t"
        "v_accvgpr_write_b32 a183, v167\n\t"
        "v_accvgpr_read_b32 v160, a184\n\t"
        "v_accvgpr_read_b32 v161, a185\n\t"
        "v_accvgpr_read_b32 v162, a186\n\t"
        "v_accvgpr_read_b32 v167, a187\n\t"
        "v_mul_f32 v160, v160, v165\n\t"
        "v_mul_f32 v161, v161, v165\n\t"
        "v_mul_f32 v162, v162, v165\n\t"
        "v_mul_f32 v167, v167, v165\n\t"
        "v_accvgpr_write_b32 a184, v160\n\t"
        "v_accvgpr_write_b32 a185, v161\n\t"
        "v_accvgpr_write_b32 a186, v162\n\t"
        "v_accvgpr_write_b32 a187, v167\n\t"
        "v_accvgpr_read_b32 v160, a188\n\t"
        "v_accvgpr_read_b32 v161, a189\n\t"
        "v_accvgpr_read_b32 v162, a190\n\t"
        "v_accvgpr_read_b32 v167, a191\n\t"
        "v_mul_f32 v160, v160, v166\n\t"
        "v_mul_f32 v161, v161, v166\n\t"
        "v_mul_f32 v162, v162, v166\n\t"
        "v_mul_f32 v167, v167, v166\n\t"
        "v_accvgpr_write_b32 a188, v160\n\t"
        "v_accvgpr_write_b32 a189, v161\n\t"
        "v_accvgpr_write_b32 a190, v162\n\t"
        "v_accvgpr_write_b32 a191, v167\n\t"
        "v_accvgpr_read_b32 v160, a192\n\t"
        "v_accvgpr_read_b32 v161, a193\n\t"
        "v_accvgpr_read_b32 v162, a194\n\t"
        "v_accvgpr_read_b32 v167, a195\n\t"
        "v_mul_f32 v160, v160, v163\n\t"
        "v_mul_f32 v161, v161, v163\n\t"
        "v_mul_f32 v162, v162, v163\n\t"
        "v_mul_f32 v167, v167, v163\n\t"
        "v_accvgpr_write_b32 a192, v160\n\t"
        "v_accvgpr_write_b32 a193, v161\n\t"
        "v_accvgpr_write_b32 a194, v162\n\t"
        "v_accvgpr_write_b32 a195, v167\n\t"
        "v_accvgpr_read_b32 v160, a196\n\t"
        "v_accvgpr_read_b32 v161, a197\n\t"
        "v_accvgpr_read_b32 v162, a198\n\t"
        "v_accvgpr_read_b32 v167, a199\n\t"
        "v_mul_f32 v160, v160, v164\n\t"
        "v_mul_f32 v161, v161, v164\n\t"
        "v_mul_f32 v162, v162, v164\n\t"
        "v_mul_f32 v167, v167, v164\n\t"
        "v_accvgpr_write_b32 a196, v160\n\t"
        "v_accvgpr_write_b32 a197, v161\n\t"
        "v_accvgpr_write_b32 a198, v162\n\t"
        "v_accvgpr_write_b32 a199, v167\n\t"
        "v_accvgpr_read_b32 v160, a200\n\t"
        "v_accvgpr_read_b32 v161, a201\n\t"
        "v_accvgpr_read_b32 v162, a202\n\t"
        "v_accvgpr_read_b32 v167, a203\n\t"
        "v_mul_f32 v160, v160, v165\n\t"
        "v_mul_f32 v161, v161, v165\n\t"
        "v_mul_f32 v162, v162, v165\n\t"
        "v_mul_f32 v167, v167, v165\n\t"
        "v_accvgpr_write_b32 a200, v160\n\t"
        "v_accvgpr_write_b32 a201, v161\n\t"
        "v_accvgpr_write_b32 a202, v162\n\t"
        "v_accvgpr_write_b32 a203, v167\n\t"
        "v_accvgpr_read_b32 v160, a204\n\t"
        "v_accvgpr_read_b32 v161, a205\n\t"
        "v_accvgpr_read_b32 v162, a206\n\t"
        "v_accvgpr_read_b32 v167, a207\n\t"
        "v_mul_f32 v160, v160, v166\n\t"
        "v_mul_f32 v161, v161, v166\n\t"
        "v_mul_f32 v162, v162, v166\n\t"
        "v_mul_f32 v167, v167, v166\n\t"
        "v_accvgpr_write_b32 a204, v160\n\t"
        "v_accvgpr_write_b32 a205, v161\n\t"
        "v_accvgpr_write_b32 a206, v162\n\t"
        "v_accvgpr_write_b32 a207, v167\n\t"
        "v_accvgpr_read_b32 v160, a208\n\t"
        "v_accvgpr_read_b32 v161, a209\n\t"
        "v_accvgpr_read_b32 v162, a210\n\t"
        "v_accvgpr_read_b32 v167, a211\n\t"
        "v_mul_f32 v160, v160, v163\n\t"
        "v_mul_f32 v161, v161, v163\n\t"
        "v_mul_f32 v162, v162, v163\n\t"
        "v_mul_f32 v167, v167, v163\n\t"
        "v_accvgpr_write_b32 a208, v160\n\t"
        "v_accvgpr_write_b32 a209, v161\n\t"
        "v_accvgpr_write_b32 a210, v162\n\t"
        "v_accvgpr_write_b32 a211, v167\n\t"
        "v_accvgpr_read_b32 v160, a212\n\t"
        "v_accvgpr_read_b32 v161, a213\n\t"
        "v_accvgpr_read_b32 v162, a214\n\t"
        "v_accvgpr_read_b32 v167, a215\n\t"
        "v_mul_f32 v160, v160, v164\n\t"
        "v_mul_f32 v161, v161, v164\n\t"
        "v_mul_f32 v162, v162, v164\n\t"
        "v_mul_f32 v167, v167, v164\n\t"
        "v_accvgpr_write_b32 a212, v160\n\t"
        "v_accvgpr_write_b32 a213, v161\n\t"
        "v_accvgpr_write_b32 a214, v162\n\t"
        "v_accvgpr_write_b32 a215, v167\n\t"
        "v_accvgpr_read_b32 v160, a216\n\t"
        "v_accvgpr_read_b32 v161, a217\n\t"
        "v_accvgpr_read_b32 v162, a218\n\t"
        "v_accvgpr_read_b32 v167, a219\n\t"
        "v_mul_f32 v160, v160, v165\n\t"
        "v_mul_f32 v161, v161, v165\n\t"
        "v_mul_f32 v162, v162, v165\n\t"
        "v_mul_f32 v167, v167, v165\n\t"
        "v_accvgpr_write_b32 a216, v160\n\t"
        "v_accvgpr_write_b32 a217, v161\n\t"
        "v_accvgpr_write_b32 a218, v162\n\t"
        "v_accvgpr_write_b32 a219, v167\n\t"
        "v_accvgpr_read_b32 v160, a220\n\t"
        "v_accvgpr_read_b32 v161, a221\n\t"
        "v_accvgpr_read_b32 v162, a222\n\t"
        "v_accvgpr_read_b32 v167, a223\n\t"
        "v_mul_f32 v160, v160, v166\n\t"
        "v_mul_f32 v161, v161, v166\n\t"
        "v_mul_f32 v162, v162, v166\n\t"
        "v_mul_f32 v167, v167, v166\n\t"
        "v_accvgpr_write_b32 a220, v160\n\t"
        "v_accvgpr_write_b32 a221, v161\n\t"
        "v_accvgpr_write_b32 a222, v162\n\t"
        "v_accvgpr_write_b32 a223, v167\n\t"
        "v_accvgpr_read_b32 v160, a224\n\t"
        "v_accvgpr_read_b32 v161, a225\n\t"
        "v_accvgpr_read_b32 v162, a226\n\t"
        "v_accvgpr_read_b32 v167, a227\n\t"
        "v_mul_f32 v160, v160, v163\n\t"
        "v_mul_f32 v161, v161, v163\n\t"
        "v_mul_f32 v162, v162, v163\n\t"
        "v_mul_f32 v167, v167, v163\n\t"
        "v_accvgpr_write_b32 a224, v160\n\t"
        "v_accvgpr_write_b32 a225, v161\n\t"
        "v_accvgpr_write_b32 a226, v162\n\t"
        "v_accvgpr_write_b32 a227, v167\n\t"
        "v_accvgpr_read_b32 v160, a228\n\t"
        "v_accvgpr_read_b32 v161, a229\n\t"
        "v_accvgpr_read_b32 v162, a230\n\t"
        "v_accvgpr_read_b32 v167, a231\n\t"
        "v_mul_f32 v160, v160, v164\n\t"
        "v_mul_f32 v161, v161, v164\n\t"
        "v_mul_f32 v162, v162, v164\n\t"
        "v_mul_f32 v167, v167, v164\n\t"
        "v_accvgpr_write_b32 a228, v160\n\t"
        "v_accvgpr_write_b32 a229, v161\n\t"
        "v_accvgpr_write_b32 a230, v162\n\t"
        "v_accvgpr_write_b32 a231, v167\n\t"
        "v_accvgpr_read_b32 v160, a232\n\t"
        "v_accvgpr_read_b32 v161, a233\n\t"
        "v_accvgpr_read_b32 v162, a234\n\t"
        "v_accvgpr_read_b32 v167, a235\n\t"
        "v_mul_f32 v160, v160, v165\n\t"
        "v_mul_f32 v161, v161, v165\n\t"
        "v_mul_f32 v162, v162, v165\n\t"
        "v_mul_f32 v167, v167, v165\n\t"
        "v_accvgpr_write_b32 a232, v160\n\t"
        "v_accvgpr_write_b32 a233, v161\n\t"
        "v_accvgpr_write_b32 a234, v162\n\t"
        "v_accvgpr_write_b32 a235, v167\n\t"
        "v_accvgpr_read_b32 v160, a236\n\t"
        "v_accvgpr_read_b32 v161, a237\n\t"
        "v_accvgpr_read_b32 v162, a238\n\t"
        "v_accvgpr_read_b32 v167, a239\n\t"
        "v_mul_f32 v160, v160, v166\n\t"
        "v_mul_f32 v161, v161, v166\n\t"
        "v_mul_f32 v162, v162, v166\n\t"
        "v_mul_f32 v167, v167, v166\n\t"
        "v_accvgpr_write_b32 a236, v160\n\t"
        "v_accvgpr_write_b32 a237, v161\n\t"
        "v_accvgpr_write_b32 a238, v162\n\t"
        "v_accvgpr_write_b32 a239, v167\n\t"
        "v_accvgpr_read_b32 v160, a240\n\t"
        "v_accvgpr_read_b32 v161, a241\n\t"
        "v_accvgpr_read_b32 v162, a242\n\t"
        "v_accvgpr_read_b32 v167, a243\n\t"
        "v_mul_f32 v160, v160, v163\n\t"
        "v_mul_f32 v161, v161, v163\n\t"
        "v_mul_f32 v162, v162, v163\n\t"
        "v_mul_f32 v167, v167, v163\n\t"
        "v_accvgpr_write_b32 a240, v160\n\t"
        "v_accvgpr_write_b32 a241, v161\n\t"
        "v_accvgpr_write_b32 a242, v162\n\t"
        "v_accvgpr_write_b32 a243, v167\n\t"
        "v_accvgpr_read_b32 v160, a244\n\t"
        "v_accvgpr_read_b32 v161, a245\n\t"
        "v_accvgpr_read_b32 v162, a246\n\t"
        "v_accvgpr_read_b32 v167, a247\n\t"
        "v_mul_f32 v160, v160, v164\n\t"
        "v_mul_f32 v161, v161, v164\n\t"
        "v_mul_f32 v162, v162, v164\n\t"
        "v_mul_f32 v167, v167, v164\n\t"
        "v_accvgpr_write_b32 a244, v160\n\t"
        "v_accvgpr_write_b32 a245, v161\n\t"
        "v_accvgpr_write_b32 a246, v162\n\t"
        "v_accvgpr_write_b32 a247, v167\n\t"
        "v_accvgpr_read_b32 v160, a248\n\t"
        "v_accvgpr_read_b32 v161, a249\n\t"
        "v_accvgpr_read_b32 v162, a250\n\t"
        "v_accvgpr_read_b32 v167, a251\n\t"
        "v_mul_f32 v160, v160, v165\n\t"
        "v_mul_f32 v161, v161, v165\n\t"
        "v_mul_f32 v162, v162, v165\n\t"
        "v_mul_f32 v167, v167, v165\n\t"
        "v_accvgpr_write_b32 a248, v160\n\t"
        "v_accvgpr_write_b32 a249, v161\n\t"
        "v_accvgpr_write_b32 a250, v162\n\t"
        "v_accvgpr_write_b32 a251, v167\n\t"
        "v_accvgpr_read_b32 v160, a252\n\t"
        "v_accvgpr_read_b32 v161, a253\n\t"
        "v_accvgpr_read_b32 v162, a254\n\t"
        "v_accvgpr_read_b32 v167, a255\n\t"
        "v_mul_f32 v160, v160, v166\n\t"
        "v_mul_f32 v161, v161, v166\n\t"
        "v_mul_f32 v162, v162, v166\n\t"
        "v_mul_f32 v167, v167, v166\n\t"
        "v_accvgpr_write_b32 a252, v160\n\t"
        "v_accvgpr_write_b32 a253, v161\n\t"
        "v_accvgpr_write_b32 a254, v162\n\t"
        "v_accvgpr_write_b32 a255, v167\n\t"
        "s_nop 3\n\t"
        "s_cmp_eq_u32 s66, 0\n\t"
        "s_cbranch_scc1 RRET0_%=\n\t"
        "s_branch RRET2_%=\n\t"
        "RSTUB0_%=:\n\t"
        "s_mov_b32 s66, 0\n\t"
        "s_branch RARE0_%=\n\t"
        "RSTUB2_%=:\n\t"
        "s_mov_b32 s66, 2\n\t"
        "s_branch RARE0_%=\n\t"
        "RARE1_%=:\n\t"
        "s_nop 7\n\t"
        "s_nop 7\n\t"
        "v_mov_b32 v128, v224\n\t"
        "v_mov_b32 v129, v224\n\t"
        "s_nop 1\n\t"
        "v_permlane16_swap_b32 v128, v129\n\t"
        "s_nop 1\n\t"
        "v_max_f32 v128, v128, v129\n\t"
        "s_nop 0\n\t"
        "v_mov_b32 v129, v128\n\t"
        "s_nop 1\n\t"
        "v_permlane32_swap_b32 v128, v129\n\t"
        "s_nop 1\n\t"
        "v_max_f32 v128, v128, v129\n\t"
        "v_mov_b32 v129, 0x7f000000\n\t"
        "v_cmp_nlt_f32 vcc, v128, v129\n\t"
        "s_nop 4\n\t"
        "s_cmp_lg_u64 vcc, 0\n\t"
        "s_cselect_b32 s71, 1, 0\n\t"
        "s_or_b32 s67, s67, s71\n\t"
        "v_frexp_exp_i32_f32 v130, v128\n\t"
        "v_max_i32 v130, 0, v130\n\t"
        "v_sub_u32 v129, 0, v130\n\t"
        "v_ldexp_f32 v131, 1.0, v129\n\t"
        "v_cvt_f32_i32 v130, v130\n\t"
        "v_sub_f32 v225, v225, v130\n\t"
        "v_mul_f32 v224, v224, v131\n\t"
        "v_mov_b32 v129, 0x4b000000\n\t"
        "v_cmp_nlt_f32 vcc, |v225|, v129\n\t"
        "s_nop 4\n\t"
        "s_cmp_lg_u64 vcc, 0\n\t"
        "s_cselect_b32 s71, 1, 0\n\t"
        "s_or_b32 s67, s67, s71\n\t"
        "v_lshlrev_b32 v128, 16, v192\n\t"
        "v_and_b32 v129, 0xffff0000, v192\n\t"
        "v_mul_f32 v128, v128, v131\n\t"
        "v_mul_f32 v129, v129, v131\n\t"
        "v_cvt_pk_bf16_f32 v192, v128, v129\n\t"
        "v_lshlrev_b32 v128, 16, v193\n\t"
        "v_and_b32 v129, 0xffff0000, v193\n\t"
        "v_mul_f32 v128, v128, v131\n\t"
        "v_mul_f32 v129, v129, v131\n\t"
        "v_cvt_pk_bf16_f32 v193, v128, v129\n\t"
        "v_lshlrev_b32 v128, 16, v194\n\t"
        "v_and_b32 v129, 0xffff0000, v194\n\t"
        "v_mul_f32 v128, v128, v131\n\t"
        "v_mul_f32 v129, v129, v131\n\t"
        "v_cvt_pk_bf16_f32 v194, v128, v129\n\t"
        "v_lshlrev_b32 v128, 16, v195\n\t"
        "v_and_b32 v129, 0xffff0000, v195\n\t"
        "v_mul_f32 v128, v128, v131\n\t"
        "v_mul_f32 v129, v129, v131\n\t"
        "v_cvt_pk_bf16_f32 v195, v128, v129\n\t"
        "v_mov_b32 v128, v226\n\t"
        "v_mov_b32 v129, v226\n\t"
        "s_nop 1\n\t"
        "v_permlane16_swap_b32 v128, v129\n\t"
        "s_nop 1\n\t"
        "v_max_f32 v128, v128, v129\n\t"
        "s_nop 0\n\t"
        "v_mov_b32 v129, v128\n\t"
        "s_nop 1\n\t"
        "v_permlane32_swap_b32 v128, v129\n\t"
        "s_nop 1\n\t"
        "v_max_f32 v128, v128, v129\n\t"
        "v_mov_b32 v129, 0x7f000000\n\t"
        "v_cmp_nlt_f32 vcc, v128, v129\n\t"
        "s_nop 4\n\t"
        "s_cmp_lg_u64 vcc, 0\n\t"
        "s_cselect_b32 s71, 1, 0\n\t"
        "s_or_b32 s67, s67, s71\n\t"
        "v_frexp_exp_i32_f32 v130, v128\n\t"
        "v_max_i32 v130, 0, v130\n\t"
        "v_sub_u32 v129, 0, v130\n\t"
        "v_ldexp_f32 v132, 1.0, v129\n\t"
        "v_cvt_f32_i32 v130, v130\n\t"
        "v_sub_f32 v227, v227, v130\n\t"
        "v_mul_f32 v226, v226, v132\n\t"
        "v_mov_b32 v129, 0x4b000000\n\t"
        "v_cmp_nlt_f32 vcc, |v227|, v129\n\t"
        "s_nop 4\n\t"
        "s_cmp_lg_u64 vcc, 0\n\t"
        "s_cselect_b32 s71, 1, 0\n\t"
        "s_or_b32 s67, s67, s71\n\t"
        "v_lshlrev_b32 v128, 16, v196\n\t"
        "v_and_b32 v129, 0xffff0000, v196\n\t"
        "v_mul_f32 v128, v128, v132\n\t"
        "v_mul_f32 v129, v129, v132\n\t"
        "v_cvt_pk_bf16_f32 v196, v128, v129\n\t"
        "v_lshlrev_b32 v128, 16, v197\n\t"
        "v_and_b32 v129, 0xffff0000, v197\n\t"
        "v_mul_f32 v128, v128, v132\n\t"
        "v_mul_f32 v129, v129, v132\n\t"
        "v_cvt_pk_bf16_f32 v197, v128, v129\n\t"
        "v_lshlrev_b32 v128, 16, v198\n\t"
        "v_and_b32 v129, 0xffff0000, v198\n\t"
        "v_mul_f32 v128, v128, v132\n\t"
        "v_mul_f32 v129, v129, v132\n\t"
        "v_cvt_pk_bf16_f32 v198, v128, v129\n\t"
        "v_lshlrev_b32 v128, 16, v199\n\t"
        "v_and_b32 v129, 0xffff0000, v199\n\t"
        "v_mul_f32 v128, v128, v132\n\t"
        "v_mul_f32 v129, v129, v132\n\t"
        "v_cvt_pk_bf16_f32 v199, v128, v129\n\t"
        "v_mov_b32 v128, v228\n\t"
        "v_mov_b32 v129, v228\n\t"
        "s_nop 1\n\t"
        "v_permlane16_swap_b32 v128, v129\n\t"
        "s_nop 1\n\t"
        "v_max_f32 v128, v128, v129\n\t"
        "s_nop 0\n\t"
        "v_mov_b32 v129, v128\n\t"
        "s_nop 1\n\t"
        "v_permlane32_swap_b32 v128, v129\n\t"
        "s_nop 1\n\t"
        "v_max_f32 v128, v128, v129\n\t"
        "v_mov_b32 v129, 0x7f000000\n\t"
        "v_cmp_nlt_f32 vcc, v128, v129\n\t"
        "s_nop 4\n\t"
        "s_cmp_lg_u64 vcc, 0\n\t"
        "s_cselect_b32 s71, 1, 0\n\t"
        "s_or_b32 s67, s67, s71\n\t"
        "v_frexp_exp_i32_f32 v130, v128\n\t"
        "v_max_i32 v130, 0, v130\n\t"
        "v_sub_u32 v129, 0, v130\n\t"
        "v_ldexp_f32 v133, 1.0, v129\n\t"
        "v_cvt_f32_i32 v130, v130\n\t"
        "v_sub_f32 v229, v229, v130\n\t"
        "v_mul_f32 v228, v228, v133\n\t"
        "v_mov_b32 v129, 0x4b000000\n\t"
        "v_cmp_nlt_f32 vcc, |v229|, v129\n\t"
        "s_nop 4\n\t"
        "s_cmp_lg_u64 vcc, 0\n\t"
        "s_cselect_b32 s71, 1, 0\n\t"
        "s_or_b32 s67, s67, s71\n\t"
        "v_lshlrev_b32 v128, 16, v200\n\t"
        "v_and_b32 v129, 0xffff0000, v200\n\t"
        "v_mul_f32 v128, v128, v133\n\t"
        "v_mul_f32 v129, v129, v133\n\t"
        "v_cvt_pk_bf16_f32 v200, v128, v129\n\t"
        "v_lshlrev_b32 v128, 16, v201\n\t"
        "v_and_b32 v129, 0xffff0000, v201\n\t"
        "v_mul_f32 v128, v128, v133\n\t"
        "v_mul_f32 v129, v129, v133\n\t"
        "v_cvt_pk_bf16_f32 v201, v128, v129\n\t"
        "v_lshlrev_b32 v128, 16, v202\n\t"
        "v_and_b32 v129, 0xffff0000, v202\n\t"
        "v_mul_f32 v128, v128, v133\n\t"
        "v_mul_f32 v129, v129, v133\n\t"
        "v_cvt_pk_bf16_f32 v202, v128, v129\n\t"
        "v_lshlrev_b32 v128, 16, v203\n\t"
        "v_and_b32 v129, 0xffff0000, v203\n\t"
        "v_mul_f32 v128, v128, v133\n\t"
        "v_mul_f32 v129, v129, v133\n\t"
        "v_cvt_pk_bf16_f32 v203, v128, v129\n\t"
        "v_mov_b32 v128, v230\n\t"
        "v_mov_b32 v129, v230\n\t"
        "s_nop 1\n\t"
        "v_permlane16_swap_b32 v128, v129\n\t"
        "s_nop 1\n\t"
        "v_max_f32 v128, v128, v129\n\t"
        "s_nop 0\n\t"
        "v_mov_b32 v129, v128\n\t"
        "s_nop 1\n\t"
        "v_permlane32_swap_b32 v128, v129\n\t"
        "s_nop 1\n\t"
        "v_max_f32 v128, v128, v129\n\t"
        "v_mov_b32 v129, 0x7f000000\n\t"
        "v_cmp_nlt_f32 vcc, v128, v129\n\t"
        "s_nop 4\n\t"
        "s_cmp_lg_u64 vcc, 0\n\t"
        "s_cselect_b32 s71, 1, 0\n\t"
        "s_or_b32 s67, s67, s71\n\t"
        "v_frexp_exp_i32_f32 v130, v128\n\t"
        "v_max_i32 v130, 0, v130\n\t"
        "v_sub_u32 v129, 0, v130\n\t"
        "v_ldexp_f32 v134, 1.0, v129\n\t"
        "v_cvt_f32_i32 v130, v130\n\t"
        "v_sub_f32 v231, v231, v130\n\t"
        "v_mul_f32 v230, v230, v134\n\t"
        "v_mov_b32 v129, 0x4b000000\n\t"
        "v_cmp_nlt_f32 vcc, |v231|, v129\n\t"
        "s_nop 4\n\t"
        "s_cmp_lg_u64 vcc, 0\n\t"
        "s_cselect_b32 s71, 1, 0\n\t"
        "s_or_b32 s67, s67, s71\n\t"
        "v_lshlrev_b32 v128, 16, v204\n\t"
        "v_and_b32 v129, 0xffff0000, v204\n\t"
        "v_mul_f32 v128, v128, v134\n\t"
        "v_mul_f32 v129, v129, v134\n\t"
        "v_cvt_pk_bf16_f32 v204, v128, v129\n\t"
        "v_lshlrev_b32 v128, 16, v205\n\t"
        "v_and_b32 v129, 0xffff0000, v205\n\t"
        "v_mul_f32 v128, v128, v134\n\t"
        "v_mul_f32 v129, v129, v134\n\t"
        "v_cvt_pk_bf16_f32 v205, v128, v129\n\t"
        "v_lshlrev_b32 v128, 16, v206\n\t"
        "v_and_b32 v129, 0xffff0000, v206\n\t"
        "v_mul_f32 v128, v128, v134\n\t"
        "v_mul_f32 v129, v129, v134\n\t"
        "v_cvt_pk_bf16_f32 v206, v128, v129\n\t"
        "v_lshlrev_b32 v128, 16, v207\n\t"
        "v_and_b32 v129, 0xffff0000, v207\n\t"
        "v_mul_f32 v128, v128, v134\n\t"
        "v_mul_f32 v129, v129, v134\n\t"
        "v_cvt_pk_bf16_f32 v207, v128, v129\n\t"
        "v_accvgpr_read_b32 v128, a0\n\t"
        "v_accvgpr_read_b32 v129, a1\n\t"
        "v_accvgpr_read_b32 v130, a2\n\t"
        "v_accvgpr_read_b32 v135, a3\n\t"
        "v_mul_f32 v128, v128, v131\n\t"
        "v_mul_f32 v129, v129, v131\n\t"
        "v_mul_f32 v130, v130, v131\n\t"
        "v_mul_f32 v135, v135, v131\n\t"
        "v_accvgpr_write_b32 a0, v128\n\t"
        "v_accvgpr_write_b32 a1, v129\n\t"
        "v_accvgpr_write_b32 a2, v130\n\t"
        "v_accvgpr_write_b32 a3, v135\n\t"
        "v_accvgpr_read_b32 v128, a4\n\t"
        "v_accvgpr_read_b32 v129, a5\n\t"
        "v_accvgpr_read_b32 v130, a6\n\t"
        "v_accvgpr_read_b32 v135, a7\n\t"
        "v_mul_f32 v128, v128, v132\n\t"
        "v_mul_f32 v129, v129, v132\n\t"
        "v_mul_f32 v130, v130, v132\n\t"
        "v_mul_f32 v135, v135, v132\n\t"
        "v_accvgpr_write_b32 a4, v128\n\t"
        "v_accvgpr_write_b32 a5, v129\n\t"
        "v_accvgpr_write_b32 a6, v130\n\t"
        "v_accvgpr_write_b32 a7, v135\n\t"
        "v_accvgpr_read_b32 v128, a8\n\t"
        "v_accvgpr_read_b32 v129, a9\n\t"
        "v_accvgpr_read_b32 v130, a10\n\t"
        "v_accvgpr_read_b32 v135, a11\n\t"
        "v_mul_f32 v128, v128, v133\n\t"
        "v_mul_f32 v129, v129, v133\n\t"
        "v_mul_f32 v130, v130, v133\n\t"
        "v_mul_f32 v135, v135, v133\n\t"
        "v_accvgpr_write_b32 a8, v128\n\t"
        "v_accvgpr_write_b32 a9, v129\n\t"
        "v_accvgpr_write_b32 a10, v130\n\t"
        "v_accvgpr_write_b32 a11, v135\n\t"
        "v_accvgpr_read_b32 v128, a12\n\t"
        "v_accvgpr_read_b32 v129, a13\n\t"
        "v_accvgpr_read_b32 v130, a14\n\t"
        "v_accvgpr_read_b32 v135, a15\n\t"
        "v_mul_f32 v128, v128, v134\n\t"
        "v_mul_f32 v129, v129, v134\n\t"
        "v_mul_f32 v130, v130, v134\n\t"
        "v_mul_f32 v135, v135, v134\n\t"
        "v_accvgpr_write_b32 a12, v128\n\t"
        "v_accvgpr_write_b32 a13, v129\n\t"
        "v_accvgpr_write_b32 a14, v130\n\t"
        "v_accvgpr_write_b32 a15, v135\n\t"
        "v_accvgpr_read_b32 v128, a16\n\t"
        "v_accvgpr_read_b32 v129, a17\n\t"
        "v_accvgpr_read_b32 v130, a18\n\t"
        "v_accvgpr_read_b32 v135, a19\n\t"
        "v_mul_f32 v128, v128, v131\n\t"
        "v_mul_f32 v129, v129, v131\n\t"
        "v_mul_f32 v130, v130, v131\n\t"
        "v_mul_f32 v135, v135, v131\n\t"
        "v_accvgpr_write_b32 a16, v128\n\t"
        "v_accvgpr_write_b32 a17, v129\n\t"
        "v_accvgpr_write_b32 a18, v130\n\t"
        "v_accvgpr_write_b32 a19, v135\n\t"
        "v_accvgpr_read_b32 v128, a20\n\t"
        "v_accvgpr_read_b32 v129, a21\n\t"
        "v_accvgpr_read_b32 v130, a22\n\t"
        "v_accvgpr_read_b32 v135, a23\n\t"
        "v_mul_f32 v128, v128, v132\n\t"
        "v_mul_f32 v129, v129, v132\n\t"
        "v_mul_f32 v130, v130, v132\n\t"
        "v_mul_f32 v135, v135, v132\n\t"
        "v_accvgpr_write_b32 a20, v128\n\t"
        "v_accvgpr_write_b32 a21, v129\n\t"
        "v_accvgpr_write_b32 a22, v130\n\t"
        "v_accvgpr_write_b32 a23, v135\n\t"
        "v_accvgpr_read_b32 v128, a24\n\t"
        "v_accvgpr_read_b32 v129, a25\n\t"
        "v_accvgpr_read_b32 v130, a26\n\t"
        "v_accvgpr_read_b32 v135, a27\n\t"
        "v_mul_f32 v128, v128, v133\n\t"
        "v_mul_f32 v129, v129, v133\n\t"
        "v_mul_f32 v130, v130, v133\n\t"
        "v_mul_f32 v135, v135, v133\n\t"
        "v_accvgpr_write_b32 a24, v128\n\t"
        "v_accvgpr_write_b32 a25, v129\n\t"
        "v_accvgpr_write_b32 a26, v130\n\t"
        "v_accvgpr_write_b32 a27, v135\n\t"
        "v_accvgpr_read_b32 v128, a28\n\t"
        "v_accvgpr_read_b32 v129, a29\n\t"
        "v_accvgpr_read_b32 v130, a30\n\t"
        "v_accvgpr_read_b32 v135, a31\n\t"
        "v_mul_f32 v128, v128, v134\n\t"
        "v_mul_f32 v129, v129, v134\n\t"
        "v_mul_f32 v130, v130, v134\n\t"
        "v_mul_f32 v135, v135, v134\n\t"
        "v_accvgpr_write_b32 a28, v128\n\t"
        "v_accvgpr_write_b32 a29, v129\n\t"
        "v_accvgpr_write_b32 a30, v130\n\t"
        "v_accvgpr_write_b32 a31, v135\n\t"
        "v_accvgpr_read_b32 v128, a32\n\t"
        "v_accvgpr_read_b32 v129, a33\n\t"
        "v_accvgpr_read_b32 v130, a34\n\t"
        "v_accvgpr_read_b32 v135, a35\n\t"
        "v_mul_f32 v128, v128, v131\n\t"
        "v_mul_f32 v129, v129, v131\n\t"
        "v_mul_f32 v130, v130, v131\n\t"
        "v_mul_f32 v135, v135, v131\n\t"
        "v_accvgpr_write_b32 a32, v128\n\t"
        "v_accvgpr_write_b32 a33, v129\n\t"
        "v_accvgpr_write_b32 a34, v130\n\t"
        "v_accvgpr_write_b32 a35, v135\n\t"
        "v_accvgpr_read_b32 v128, a36\n\t"
        "v_accvgpr_read_b32 v129, a37\n\t"
        "v_accvgpr_read_b32 v130, a38\n\t"
        "v_accvgpr_read_b32 v135, a39\n\t"
        "v_mul_f32 v128, v128, v132\n\t"
        "v_mul_f32 v129, v129, v132\n\t"
        "v_mul_f32 v130, v130, v132\n\t"
        "v_mul_f32 v135, v135, v132\n\t"
        "v_accvgpr_write_b32 a36, v128\n\t"
        "v_accvgpr_write_b32 a37, v129\n\t"
        "v_accvgpr_write_b32 a38, v130\n\t"
        "v_accvgpr_write_b32 a39, v135\n\t"
        "v_accvgpr_read_b32 v128, a40\n\t"
        "v_accvgpr_read_b32 v129, a41\n\t"
        "v_accvgpr_read_b32 v130, a42\n\t"
        "v_accvgpr_read_b32 v135, a43\n\t"
        "v_mul_f32 v128, v128, v133\n\t"
        "v_mul_f32 v129, v129, v133\n\t"
        "v_mul_f32 v130, v130, v133\n\t"
        "v_mul_f32 v135, v135, v133\n\t"
        "v_accvgpr_write_b32 a40, v128\n\t"
        "v_accvgpr_write_b32 a41, v129\n\t"
        "v_accvgpr_write_b32 a42, v130\n\t"
        "v_accvgpr_write_b32 a43, v135\n\t"
        "v_accvgpr_read_b32 v128, a44\n\t"
        "v_accvgpr_read_b32 v129, a45\n\t"
        "v_accvgpr_read_b32 v130, a46\n\t"
        "v_accvgpr_read_b32 v135, a47\n\t"
        "v_mul_f32 v128, v128, v134\n\t"
        "v_mul_f32 v129, v129, v134\n\t"
        "v_mul_f32 v130, v130, v134\n\t"
        "v_mul_f32 v135, v135, v134\n\t"
        "v_accvgpr_write_b32 a44, v128\n\t"
        "v_accvgpr_write_b32 a45, v129\n\t"
        "v_accvgpr_write_b32 a46, v130\n\t"
        "v_accvgpr_write_b32 a47, v135\n\t"
        "v_accvgpr_read_b32 v128, a48\n\t"
        "v_accvgpr_read_b32 v129, a49\n\t"
        "v_accvgpr_read_b32 v130, a50\n\t"
        "v_accvgpr_read_b32 v135, a51\n\t"
        "v_mul_f32 v128, v128, v131\n\t"
        "v_mul_f32 v129, v129, v131\n\t"
        "v_mul_f32 v130, v130, v131\n\t"
        "v_mul_f32 v135, v135, v131\n\t"
        "v_accvgpr_write_b32 a48, v128\n\t"
        "v_accvgpr_write_b32 a49, v129\n\t"
        "v_accvgpr_write_b32 a50, v130\n\t"
        "v_accvgpr_write_b32 a51, v135\n\t"
        "v_accvgpr_read_b32 v128, a52\n\t"
        "v_accvgpr_read_b32 v129, a53\n\t"
        "v_accvgpr_read_b32 v130, a54\n\t"
        "v_accvgpr_read_b32 v135, a55\n\t"
        "v_mul_f32 v128, v128, v132\n\t"
        "v_mul_f32 v129, v129, v132\n\t"
        "v_mul_f32 v130, v130, v132\n\t"
        "v_mul_f32 v135, v135, v132\n\t"
        "v_accvgpr_write_b32 a52, v128\n\t"
        "v_accvgpr_write_b32 a53, v129\n\t"
        "v_accvgpr_write_b32 a54, v130\n\t"
        "v_accvgpr_write_b32 a55, v135\n\t"
        "v_accvgpr_read_b32 v128, a56\n\t"
        "v_accvgpr_read_b32 v129, a57\n\t"
        "v_accvgpr_read_b32 v130, a58\n\t"
        "v_accvgpr_read_b32 v135, a59\n\t"
        "v_mul_f32 v128, v128, v133\n\t"
        "v_mul_f32 v129, v129, v133\n\t"
        "v_mul_f32 v130, v130, v133\n\t"
        "v_mul_f32 v135, v135, v133\n\t"
        "v_accvgpr_write_b32 a56, v128\n\t"
        "v_accvgpr_write_b32 a57, v129\n\t"
        "v_accvgpr_write_b32 a58, v130\n\t"
        "v_accvgpr_write_b32 a59, v135\n\t"
        "v_accvgpr_read_b32 v128, a60\n\t"
        "v_accvgpr_read_b32 v129, a61\n\t"
        "v_accvgpr_read_b32 v130, a62\n\t"
        "v_accvgpr_read_b32 v135, a63\n\t"
        "v_mul_f32 v128, v128, v134\n\t"
        "v_mul_f32 v129, v129, v134\n\t"
        "v_mul_f32 v130, v130, v134\n\t"
        "v_mul_f32 v135, v135, v134\n\t"
        "v_accvgpr_write_b32 a60, v128\n\t"
        "v_accvgpr_write_b32 a61, v129\n\t"
        "v_accvgpr_write_b32 a62, v130\n\t"
        "v_accvgpr_write_b32 a63, v135\n\t"
        "v_accvgpr_read_b32 v128, a64\n\t"
        "v_accvgpr_read_b32 v129, a65\n\t"
        "v_accvgpr_read_b32 v130, a66\n\t"
        "v_accvgpr_read_b32 v135, a67\n\t"
        "v_mul_f32 v128, v128, v131\n\t"
        "v_mul_f32 v129, v129, v131\n\t"
        "v_mul_f32 v130, v130, v131\n\t"
        "v_mul_f32 v135, v135, v131\n\t"
        "v_accvgpr_write_b32 a64, v128\n\t"
        "v_accvgpr_write_b32 a65, v129\n\t"
        "v_accvgpr_write_b32 a66, v130\n\t"
        "v_accvgpr_write_b32 a67, v135\n\t"
        "v_accvgpr_read_b32 v128, a68\n\t"
        "v_accvgpr_read_b32 v129, a69\n\t"
        "v_accvgpr_read_b32 v130, a70\n\t"
        "v_accvgpr_read_b32 v135, a71\n\t"
        "v_mul_f32 v128, v128, v132\n\t"
        "v_mul_f32 v129, v129, v132\n\t"
        "v_mul_f32 v130, v130, v132\n\t"
        "v_mul_f32 v135, v135, v132\n\t"
        "v_accvgpr_write_b32 a68, v128\n\t"
        "v_accvgpr_write_b32 a69, v129\n\t"
        "v_accvgpr_write_b32 a70, v130\n\t"
        "v_accvgpr_write_b32 a71, v135\n\t"
        "v_accvgpr_read_b32 v128, a72\n\t"
        "v_accvgpr_read_b32 v129, a73\n\t"
        "v_accvgpr_read_b32 v130, a74\n\t"
        "v_accvgpr_read_b32 v135, a75\n\t"
        "v_mul_f32 v128, v128, v133\n\t"
        "v_mul_f32 v129, v129, v133\n\t"
        "v_mul_f32 v130, v130, v133\n\t"
        "v_mul_f32 v135, v135, v133\n\t"
        "v_accvgpr_write_b32 a72, v128\n\t"
        "v_accvgpr_write_b32 a73, v129\n\t"
        "v_accvgpr_write_b32 a74, v130\n\t"
        "v_accvgpr_write_b32 a75, v135\n\t"
        "v_accvgpr_read_b32 v128, a76\n\t"
        "v_accvgpr_read_b32 v129, a77\n\t"
        "v_accvgpr_read_b32 v130, a78\n\t"
        "v_accvgpr_read_b32 v135, a79\n\t"
        "v_mul_f32 v128, v128, v134\n\t"
        "v_mul_f32 v129, v129, v134\n\t"
        "v_mul_f32 v130, v130, v134\n\t"
        "v_mul_f32 v135, v135, v134\n\t"
        "v_accvgpr_write_b32 a76, v128\n\t"
        "v_accvgpr_write_b32 a77, v129\n\t"
        "v_accvgpr_write_b32 a78, v130\n\t"
        "v_accvgpr_write_b32 a79, v135\n\t"
        "v_accvgpr_read_b32 v128, a80\n\t"
        "v_accvgpr_read_b32 v129, a81\n\t"
        "v_accvgpr_read_b32 v130, a82\n\t"
        "v_accvgpr_read_b32 v135, a83\n\t"
        "v_mul_f32 v128, v128, v131\n\t"
        "v_mul_f32 v129, v129, v131\n\t"
        "v_mul_f32 v130, v130, v131\n\t"
        "v_mul_f32 v135, v135, v131\n\t"
        "v_accvgpr_write_b32 a80, v128\n\t"
        "v_accvgpr_write_b32 a81, v129\n\t"
        "v_accvgpr_write_b32 a82, v130\n\t"
        "v_accvgpr_write_b32 a83, v135\n\t"
        "v_accvgpr_read_b32 v128, a84\n\t"
        "v_accvgpr_read_b32 v129, a85\n\t"
        "v_accvgpr_read_b32 v130, a86\n\t"
        "v_accvgpr_read_b32 v135, a87\n\t"
        "v_mul_f32 v128, v128, v132\n\t"
        "v_mul_f32 v129, v129, v132\n\t"
        "v_mul_f32 v130, v130, v132\n\t"
        "v_mul_f32 v135, v135, v132\n\t"
        "v_accvgpr_write_b32 a84, v128\n\t"
        "v_accvgpr_write_b32 a85, v129\n\t"
        "v_accvgpr_write_b32 a86, v130\n\t"
        "v_accvgpr_write_b32 a87, v135\n\t"
        "v_accvgpr_read_b32 v128, a88\n\t"
        "v_accvgpr_read_b32 v129, a89\n\t"
        "v_accvgpr_read_b32 v130, a90\n\t"
        "v_accvgpr_read_b32 v135, a91\n\t"
        "v_mul_f32 v128, v128, v133\n\t"
        "v_mul_f32 v129, v129, v133\n\t"
        "v_mul_f32 v130, v130, v133\n\t"
        "v_mul_f32 v135, v135, v133\n\t"
        "v_accvgpr_write_b32 a88, v128\n\t"
        "v_accvgpr_write_b32 a89, v129\n\t"
        "v_accvgpr_write_b32 a90, v130\n\t"
        "v_accvgpr_write_b32 a91, v135\n\t"
        "v_accvgpr_read_b32 v128, a92\n\t"
        "v_accvgpr_read_b32 v129, a93\n\t"
        "v_accvgpr_read_b32 v130, a94\n\t"
        "v_accvgpr_read_b32 v135, a95\n\t"
        "v_mul_f32 v128, v128, v134\n\t"
        "v_mul_f32 v129, v129, v134\n\t"
        "v_mul_f32 v130, v130, v134\n\t"
        "v_mul_f32 v135, v135, v134\n\t"
        "v_accvgpr_write_b32 a92, v128\n\t"
        "v_accvgpr_write_b32 a93, v129\n\t"
        "v_accvgpr_write_b32 a94, v130\n\t"
        "v_accvgpr_write_b32 a95, v135\n\t"
        "v_accvgpr_read_b32 v128, a96\n\t"
        "v_accvgpr_read_b32 v129, a97\n\t"
        "v_accvgpr_read_b32 v130, a98\n\t"
        "v_accvgpr_read_b32 v135, a99\n\t"
        "v_mul_f32 v128, v128, v131\n\t"
        "v_mul_f32 v129, v129, v131\n\t"
        "v_mul_f32 v130, v130, v131\n\t"
        "v_mul_f32 v135, v135, v131\n\t"
        "v_accvgpr_write_b32 a96, v128\n\t"
        "v_accvgpr_write_b32 a97, v129\n\t"
        "v_accvgpr_write_b32 a98, v130\n\t"
        "v_accvgpr_write_b32 a99, v135\n\t"
        "v_accvgpr_read_b32 v128, a100\n\t"
        "v_accvgpr_read_b32 v129, a101\n\t"
        "v_accvgpr_read_b32 v130, a102\n\t"
        "v_accvgpr_read_b32 v135, a103\n\t"
        "v_mul_f32 v128, v128, v132\n\t"
        "v_mul_f32 v129, v129, v132\n\t"
        "v_mul_f32 v130, v130, v132\n\t"
        "v_mul_f32 v135, v135, v132\n\t"
        "v_accvgpr_write_b32 a100, v128\n\t"
        "v_accvgpr_write_b32 a101, v129\n\t"
        "v_accvgpr_write_b32 a102, v130\n\t"
        "v_accvgpr_write_b32 a103, v135\n\t"
        "v_accvgpr_read_b32 v128, a104\n\t"
        "v_accvgpr_read_b32 v129, a105\n\t"
        "v_accvgpr_read_b32 v130, a106\n\t"
        "v_accvgpr_read_b32 v135, a107\n\t"
        "v_mul_f32 v128, v128, v133\n\t"
        "v_mul_f32 v129, v129, v133\n\t"
        "v_mul_f32 v130, v130, v133\n\t"
        "v_mul_f32 v135, v135, v133\n\t"
        "v_accvgpr_write_b32 a104, v128\n\t"
        "v_accvgpr_write_b32 a105, v129\n\t"
        "v_accvgpr_write_b32 a106, v130\n\t"
        "v_accvgpr_write_b32 a107, v135\n\t"
        "v_accvgpr_read_b32 v128, a108\n\t"
        "v_accvgpr_read_b32 v129, a109\n\t"
        "v_accvgpr_read_b32 v130, a110\n\t"
        "v_accvgpr_read_b32 v135, a111\n\t"
        "v_mul_f32 v128, v128, v134\n\t"
        "v_mul_f32 v129, v129, v134\n\t"
        "v_mul_f32 v130, v130, v134\n\t"
        "v_mul_f32 v135, v135, v134\n\t"
        "v_accvgpr_write_b32 a108, v128\n\t"
        "v_accvgpr_write_b32 a109, v129\n\t"
        "v_accvgpr_write_b32 a110, v130\n\t"
        "v_accvgpr_write_b32 a111, v135\n\t"
        "v_accvgpr_read_b32 v128, a112\n\t"
        "v_accvgpr_read_b32 v129, a113\n\t"
        "v_accvgpr_read_b32 v130, a114\n\t"
        "v_accvgpr_read_b32 v135, a115\n\t"
        "v_mul_f32 v128, v128, v131\n\t"
        "v_mul_f32 v129, v129, v131\n\t"
        "v_mul_f32 v130, v130, v131\n\t"
        "v_mul_f32 v135, v135, v131\n\t"
        "v_accvgpr_write_b32 a112, v128\n\t"
        "v_accvgpr_write_b32 a113, v129\n\t"
        "v_accvgpr_write_b32 a114, v130\n\t"
        "v_accvgpr_write_b32 a115, v135\n\t"
        "v_accvgpr_read_b32 v128, a116\n\t"
        "v_accvgpr_read_b32 v129, a117\n\t"
        "v_accvgpr_read_b32 v130, a118\n\t"
        "v_accvgpr_read_b32 v135, a119\n\t"
        "v_mul_f32 v128, v128, v132\n\t"
        "v_mul_f32 v129, v129, v132\n\t"
        "v_mul_f32 v130, v130, v132\n\t"
        "v_mul_f32 v135, v135, v132\n\t"
        "v_accvgpr_write_b32 a116, v128\n\t"
        "v_accvgpr_write_b32 a117, v129\n\t"
        "v_accvgpr_write_b32 a118, v130\n\t"
        "v_accvgpr_write_b32 a119, v135\n\t"
        "v_accvgpr_read_b32 v128, a120\n\t"
        "v_accvgpr_read_b32 v129, a121\n\t"
        "v_accvgpr_read_b32 v130, a122\n\t"
        "v_accvgpr_read_b32 v135, a123\n\t"
        "v_mul_f32 v128, v128, v133\n\t"
        "v_mul_f32 v129, v129, v133\n\t"
        "v_mul_f32 v130, v130, v133\n\t"
        "v_mul_f32 v135, v135, v133\n\t"
        "v_accvgpr_write_b32 a120, v128\n\t"
        "v_accvgpr_write_b32 a121, v129\n\t"
        "v_accvgpr_write_b32 a122, v130\n\t"
        "v_accvgpr_write_b32 a123, v135\n\t"
        "v_accvgpr_read_b32 v128, a124\n\t"
        "v_accvgpr_read_b32 v129, a125\n\t"
        "v_accvgpr_read_b32 v130, a126\n\t"
        "v_accvgpr_read_b32 v135, a127\n\t"
        "v_mul_f32 v128, v128, v134\n\t"
        "v_mul_f32 v129, v129, v134\n\t"
        "v_mul_f32 v130, v130, v134\n\t"
        "v_mul_f32 v135, v135, v134\n\t"
        "v_accvgpr_write_b32 a124, v128\n\t"
        "v_accvgpr_write_b32 a125, v129\n\t"
        "v_accvgpr_write_b32 a126, v130\n\t"
        "v_accvgpr_write_b32 a127, v135\n\t"
        "v_accvgpr_read_b32 v128, a128\n\t"
        "v_accvgpr_read_b32 v129, a129\n\t"
        "v_accvgpr_read_b32 v130, a130\n\t"
        "v_accvgpr_read_b32 v135, a131\n\t"
        "v_mul_f32 v128, v128, v131\n\t"
        "v_mul_f32 v129, v129, v131\n\t"
        "v_mul_f32 v130, v130, v131\n\t"
        "v_mul_f32 v135, v135, v131\n\t"
        "v_accvgpr_write_b32 a128, v128\n\t"
        "v_accvgpr_write_b32 a129, v129\n\t"
        "v_accvgpr_write_b32 a130, v130\n\t"
        "v_accvgpr_write_b32 a131, v135\n\t"
        "v_accvgpr_read_b32 v128, a132\n\t"
        "v_accvgpr_read_b32 v129, a133\n\t"
        "v_accvgpr_read_b32 v130, a134\n\t"
        "v_accvgpr_read_b32 v135, a135\n\t"
        "v_mul_f32 v128, v128, v132\n\t"
        "v_mul_f32 v129, v129, v132\n\t"
        "v_mul_f32 v130, v130, v132\n\t"
        "v_mul_f32 v135, v135, v132\n\t"
        "v_accvgpr_write_b32 a132, v128\n\t"
        "v_accvgpr_write_b32 a133, v129\n\t"
        "v_accvgpr_write_b32 a134, v130\n\t"
        "v_accvgpr_write_b32 a135, v135\n\t"
        "v_accvgpr_read_b32 v128, a136\n\t"
        "v_accvgpr_read_b32 v129, a137\n\t"
        "v_accvgpr_read_b32 v130, a138\n\t"
        "v_accvgpr_read_b32 v135, a139\n\t"
        "v_mul_f32 v128, v128, v133\n\t"
        "v_mul_f32 v129, v129, v133\n\t"
        "v_mul_f32 v130, v130, v133\n\t"
        "v_mul_f32 v135, v135, v133\n\t"
        "v_accvgpr_write_b32 a136, v128\n\t"
        "v_accvgpr_write_b32 a137, v129\n\t"
        "v_accvgpr_write_b32 a138, v130\n\t"
        "v_accvgpr_write_b32 a139, v135\n\t"
        "v_accvgpr_read_b32 v128, a140\n\t"
        "v_accvgpr_read_b32 v129, a141\n\t"
        "v_accvgpr_read_b32 v130, a142\n\t"
        "v_accvgpr_read_b32 v135, a143\n\t"
        "v_mul_f32 v128, v128, v134\n\t"
        "v_mul_f32 v129, v129, v134\n\t"
        "v_mul_f32 v130, v130, v134\n\t"
        "v_mul_f32 v135, v135, v134\n\t"
        "v_accvgpr_write_b32 a140, v128\n\t"
        "v_accvgpr_write_b32 a141, v129\n\t"
        "v_accvgpr_write_b32 a142, v130\n\t"
        "v_accvgpr_write_b32 a143, v135\n\t"
        "v_accvgpr_read_b32 v128, a144\n\t"
        "v_accvgpr_read_b32 v129, a145\n\t"
        "v_accvgpr_read_b32 v130, a146\n\t"
        "v_accvgpr_read_b32 v135, a147\n\t"
        "v_mul_f32 v128, v128, v131\n\t"
        "v_mul_f32 v129, v129, v131\n\t"
        "v_mul_f32 v130, v130, v131\n\t"
        "v_mul_f32 v135, v135, v131\n\t"
        "v_accvgpr_write_b32 a144, v128\n\t"
        "v_accvgpr_write_b32 a145, v129\n\t"
        "v_accvgpr_write_b32 a146, v130\n\t"
        "v_accvgpr_write_b32 a147, v135\n\t"
        "v_accvgpr_read_b32 v128, a148\n\t"
        "v_accvgpr_read_b32 v129, a149\n\t"
        "v_accvgpr_read_b32 v130, a150\n\t"
        "v_accvgpr_read_b32 v135, a151\n\t"
        "v_mul_f32 v128, v128, v132\n\t"
        "v_mul_f32 v129, v129, v132\n\t"
        "v_mul_f32 v130, v130, v132\n\t"
        "v_mul_f32 v135, v135, v132\n\t"
        "v_accvgpr_write_b32 a148, v128\n\t"
        "v_accvgpr_write_b32 a149, v129\n\t"
        "v_accvgpr_write_b32 a150, v130\n\t"
        "v_accvgpr_write_b32 a151, v135\n\t"
        "v_accvgpr_read_b32 v128, a152\n\t"
        "v_accvgpr_read_b32 v129, a153\n\t"
        "v_accvgpr_read_b32 v130, a154\n\t"
        "v_accvgpr_read_b32 v135, a155\n\t"
        "v_mul_f32 v128, v128, v133\n\t"
        "v_mul_f32 v129, v129, v133\n\t"
        "v_mul_f32 v130, v130, v133\n\t"
        "v_mul_f32 v135, v135, v133\n\t"
        "v_accvgpr_write_b32 a152, v128\n\t"
        "v_accvgpr_write_b32 a153, v129\n\t"
        "v_accvgpr_write_b32 a154, v130\n\t"
        "v_accvgpr_write_b32 a155, v135\n\t"
        "v_accvgpr_read_b32 v128, a156\n\t"
        "v_accvgpr_read_b32 v129, a157\n\t"
        "v_accvgpr_read_b32 v130, a158\n\t"
        "v_accvgpr_read_b32 v135, a159\n\t"
        "v_mul_f32 v128, v128, v134\n\t"
        "v_mul_f32 v129, v129, v134\n\t"
        "v_mul_f32 v130, v130, v134\n\t"
        "v_mul_f32 v135, v135, v134\n\t"
        "v_accvgpr_write_b32 a156, v128\n\t"
        "v_accvgpr_write_b32 a157, v129\n\t"
        "v_accvgpr_write_b32 a158, v130\n\t"
        "v_accvgpr_write_b32 a159, v135\n\t"
        "v_accvgpr_read_b32 v128, a160\n\t"
        "v_accvgpr_read_b32 v129, a161\n\t"
        "v_accvgpr_read_b32 v130, a162\n\t"
        "v_accvgpr_read_b32 v135, a163\n\t"
        "v_mul_f32 v128, v128, v131\n\t"
        "v_mul_f32 v129, v129, v131\n\t"
        "v_mul_f32 v130, v130, v131\n\t"
        "v_mul_f32 v135, v135, v131\n\t"
        "v_accvgpr_write_b32 a160, v128\n\t"
        "v_accvgpr_write_b32 a161, v129\n\t"
        "v_accvgpr_write_b32 a162, v130\n\t"
        "v_accvgpr_write_b32 a163, v135\n\t"
        "v_accvgpr_read_b32 v128, a164\n\t"
        "v_accvgpr_read_b32 v129, a165\n\t"
        "v_accvgpr_read_b32 v130, a166\n\t"
        "v_accvgpr_read_b32 v135, a167\n\t"
        "v_mul_f32 v128, v128, v132\n\t"
        "v_mul_f32 v129, v129, v132\n\t"
        "v_mul_f32 v130, v130, v132\n\t"
        "v_mul_f32 v135, v135, v132\n\t"
        "v_accvgpr_write_b32 a164, v128\n\t"
        "v_accvgpr_write_b32 a165, v129\n\t"
        "v_accvgpr_write_b32 a166, v130\n\t"
        "v_accvgpr_write_b32 a167, v135\n\t"
        "v_accvgpr_read_b32 v128, a168\n\t"
        "v_accvgpr_read_b32 v129, a169\n\t"
        "v_accvgpr_read_b32 v130, a170\n\t"
        "v_accvgpr_read_b32 v135, a171\n\t"
        "v_mul_f32 v128, v128, v133\n\t"
        "v_mul_f32 v129, v129, v133\n\t"
        "v_mul_f32 v130, v130, v133\n\t"
        "v_mul_f32 v135, v135, v133\n\t"
        "v_accvgpr_write_b32 a168, v128\n\t"
        "v_accvgpr_write_b32 a169, v129\n\t"
        "v_accvgpr_write_b32 a170, v130\n\t"
        "v_accvgpr_write_b32 a171, v135\n\t"
        "v_accvgpr_read_b32 v128, a172\n\t"
        "v_accvgpr_read_b32 v129, a173\n\t"
        "v_accvgpr_read_b32 v130, a174\n\t"
        "v_accvgpr_read_b32 v135, a175\n\t"
        "v_mul_f32 v128, v128, v134\n\t"
        "v_mul_f32 v129, v129, v134\n\t"
        "v_mul_f32 v130, v130, v134\n\t"
        "v_mul_f32 v135, v135, v134\n\t"
        "v_accvgpr_write_b32 a172, v128\n\t"
        "v_accvgpr_write_b32 a173, v129\n\t"
        "v_accvgpr_write_b32 a174, v130\n\t"
        "v_accvgpr_write_b32 a175, v135\n\t"
        "v_accvgpr_read_b32 v128, a176\n\t"
        "v_accvgpr_read_b32 v129, a177\n\t"
        "v_accvgpr_read_b32 v130, a178\n\t"
        "v_accvgpr_read_b32 v135, a179\n\t"
        "v_mul_f32 v128, v128, v131\n\t"
        "v_mul_f32 v129, v129, v131\n\t"
        "v_mul_f32 v130, v130, v131\n\t"
        "v_mul_f32 v135, v135, v131\n\t"
        "v_accvgpr_write_b32 a176, v128\n\t"
        "v_accvgpr_write_b32 a177, v129\n\t"
        "v_accvgpr_write_b32 a178, v130\n\t"
        "v_accvgpr_write_b32 a179, v135\n\t"
        "v_accvgpr_read_b32 v128, a180\n\t"
        "v_accvgpr_read_b32 v129, a181\n\t"
        "v_accvgpr_read_b32 v130, a182\n\t"
        "v_accvgpr_read_b32 v135, a183\n\t"
        "v_mul_f32 v128, v128, v132\n\t"
        "v_mul_f32 v129, v129, v132\n\t"
        "v_mul_f32 v130, v130, v132\n\t"
        "v_mul_f32 v135, v135, v132\n\t"
        "v_accvgpr_write_b32 a180, v128\n\t"
        "v_accvgpr_write_b32 a181, v129\n\t"
        "v_accvgpr_write_b32 a182, v130\n\t"
        "v_accvgpr_write_b32 a183, v135\n\t"
        "v_accvgpr_read_b32 v128, a184\n\t"
        "v_accvgpr_read_b32 v129, a185\n\t"
        "v_accvgpr_read_b32 v130, a186\n\t"
        "v_accvgpr_read_b32 v135, a187\n\t"
        "v_mul_f32 v128, v128, v133\n\t"
        "v_mul_f32 v129, v129, v133\n\t"
        "v_mul_f32 v130, v130, v133\n\t"
        "v_mul_f32 v135, v135, v133\n\t"
        "v_accvgpr_write_b32 a184, v128\n\t"
        "v_accvgpr_write_b32 a185, v129\n\t"
        "v_accvgpr_write_b32 a186, v130\n\t"
        "v_accvgpr_write_b32 a187, v135\n\t"
        "v_accvgpr_read_b32 v128, a188\n\t"
        "v_accvgpr_read_b32 v129, a189\n\t"
        "v_accvgpr_read_b32 v130, a190\n\t"
        "v_accvgpr_read_b32 v135, a191\n\t"
        "v_mul_f32 v128, v128, v134\n\t"
        "v_mul_f32 v129, v129, v134\n\t"
        "v_mul_f32 v130, v130, v134\n\t"
        "v_mul_f32 v135, v135, v134\n\t"
        "v_accvgpr_write_b32 a188, v128\n\t"
        "v_accvgpr_write_b32 a189, v129\n\t"
        "v_accvgpr_write_b32 a190, v130\n\t"
        "v_accvgpr_write_b32 a191, v135\n\t"
        "v_accvgpr_read_b32 v128, a192\n\t"
        "v_accvgpr_read_b32 v129, a193\n\t"
        "v_accvgpr_read_b32 v130, a194\n\t"
        "v_accvgpr_read_b32 v135, a195\n\t"
        "v_mul_f32 v128, v128, v131\n\t"
        "v_mul_f32 v129, v129, v131\n\t"
        "v_mul_f32 v130, v130, v131\n\t"
        "v_mul_f32 v135, v135, v131\n\t"
        "v_accvgpr_write_b32 a192, v128\n\t"
        "v_accvgpr_write_b32 a193, v129\n\t"
        "v_accvgpr_write_b32 a194, v130\n\t"
        "v_accvgpr_write_b32 a195, v135\n\t"
        "v_accvgpr_read_b32 v128, a196\n\t"
        "v_accvgpr_read_b32 v129, a197\n\t"
        "v_accvgpr_read_b32 v130, a198\n\t"
        "v_accvgpr_read_b32 v135, a199\n\t"
        "v_mul_f32 v128, v128, v132\n\t"
        "v_mul_f32 v129, v129, v132\n\t"
        "v_mul_f32 v130, v130, v132\n\t"
        "v_mul_f32 v135, v135, v132\n\t"
        "v_accvgpr_write_b32 a196, v128\n\t"
        "v_accvgpr_write_b32 a197, v129\n\t"
        "v_accvgpr_write_b32 a198, v130\n\t"
        "v_accvgpr_write_b32 a199, v135\n\t"
        "v_accvgpr_read_b32 v128, a200\n\t"
        "v_accvgpr_read_b32 v129, a201\n\t"
        "v_accvgpr_read_b32 v130, a202\n\t"
        "v_accvgpr_read_b32 v135, a203\n\t"
        "v_mul_f32 v128, v128, v133\n\t"
        "v_mul_f32 v129, v129, v133\n\t"
        "v_mul_f32 v130, v130, v133\n\t"
        "v_mul_f32 v135, v135, v133\n\t"
        "v_accvgpr_write_b32 a200, v128\n\t"
        "v_accvgpr_write_b32 a201, v129\n\t"
        "v_accvgpr_write_b32 a202, v130\n\t"
        "v_accvgpr_write_b32 a203, v135\n\t"
        "v_accvgpr_read_b32 v128, a204\n\t"
        "v_accvgpr_read_b32 v129, a205\n\t"
        "v_accvgpr_read_b32 v130, a206\n\t"
        "v_accvgpr_read_b32 v135, a207\n\t"
        "v_mul_f32 v128, v128, v134\n\t"
        "v_mul_f32 v129, v129, v134\n\t"
        "v_mul_f32 v130, v130, v134\n\t"
        "v_mul_f32 v135, v135, v134\n\t"
        "v_accvgpr_write_b32 a204, v128\n\t"
        "v_accvgpr_write_b32 a205, v129\n\t"
        "v_accvgpr_write_b32 a206, v130\n\t"
        "v_accvgpr_write_b32 a207, v135\n\t"
        "v_accvgpr_read_b32 v128, a208\n\t"
        "v_accvgpr_read_b32 v129, a209\n\t"
        "v_accvgpr_read_b32 v130, a210\n\t"
        "v_accvgpr_read_b32 v135, a211\n\t"
        "v_mul_f32 v128, v128, v131\n\t"
        "v_mul_f32 v129, v129, v131\n\t"
        "v_mul_f32 v130, v130, v131\n\t"
        "v_mul_f32 v135, v135, v131\n\t"
        "v_accvgpr_write_b32 a208, v128\n\t"
        "v_accvgpr_write_b32 a209, v129\n\t"
        "v_accvgpr_write_b32 a210, v130\n\t"
        "v_accvgpr_write_b32 a211, v135\n\t"
        "v_accvgpr_read_b32 v128, a212\n\t"
        "v_accvgpr_read_b32 v129, a213\n\t"
        "v_accvgpr_read_b32 v130, a214\n\t"
        "v_accvgpr_read_b32 v135, a215\n\t"
        "v_mul_f32 v128, v128, v132\n\t"
        "v_mul_f32 v129, v129, v132\n\t"
        "v_mul_f32 v130, v130, v132\n\t"
        "v_mul_f32 v135, v135, v132\n\t"
        "v_accvgpr_write_b32 a212, v128\n\t"
        "v_accvgpr_write_b32 a213, v129\n\t"
        "v_accvgpr_write_b32 a214, v130\n\t"
        "v_accvgpr_write_b32 a215, v135\n\t"
        "v_accvgpr_read_b32 v128, a216\n\t"
        "v_accvgpr_read_b32 v129, a217\n\t"
        "v_accvgpr_read_b32 v130, a218\n\t"
        "v_accvgpr_read_b32 v135, a219\n\t"
        "v_mul_f32 v128, v128, v133\n\t"
        "v_mul_f32 v129, v129, v133\n\t"
        "v_mul_f32 v130, v130, v133\n\t"
        "v_mul_f32 v135, v135, v133\n\t"
        "v_accvgpr_write_b32 a216, v128\n\t"
        "v_accvgpr_write_b32 a217, v129\n\t"
        "v_accvgpr_write_b32 a218, v130\n\t"
        "v_accvgpr_write_b32 a219, v135\n\t"
        "v_accvgpr_read_b32 v128, a220\n\t"
        "v_accvgpr_read_b32 v129, a221\n\t"
        "v_accvgpr_read_b32 v130, a222\n\t"
        "v_accvgpr_read_b32 v135, a223\n\t"
        "v_mul_f32 v128, v128, v134\n\t"
        "v_mul_f32 v129, v129, v134\n\t"
        "v_mul_f32 v130, v130, v134\n\t"
        "v_mul_f32 v135, v135, v134\n\t"
        "v_accvgpr_write_b32 a220, v128\n\t"
        "v_accvgpr_write_b32 a221, v129\n\t"
        "v_accvgpr_write_b32 a222, v130\n\t"
        "v_accvgpr_write_b32 a223, v135\n\t"
        "v_accvgpr_read_b32 v128, a224\n\t"
        "v_accvgpr_read_b32 v129, a225\n\t"
        "v_accvgpr_read_b32 v130, a226\n\t"
        "v_accvgpr_read_b32 v135, a227\n\t"
        "v_mul_f32 v128, v128, v131\n\t"
        "v_mul_f32 v129, v129, v131\n\t"
        "v_mul_f32 v130, v130, v131\n\t"
        "v_mul_f32 v135, v135, v131\n\t"
        "v_accvgpr_write_b32 a224, v128\n\t"
        "v_accvgpr_write_b32 a225, v129\n\t"
        "v_accvgpr_write_b32 a226, v130\n\t"
        "v_accvgpr_write_b32 a227, v135\n\t"
        "v_accvgpr_read_b32 v128, a228\n\t"
        "v_accvgpr_read_b32 v129, a229\n\t"
        "v_accvgpr_read_b32 v130, a230\n\t"
        "v_accvgpr_read_b32 v135, a231\n\t"
        "v_mul_f32 v128, v128, v132\n\t"
        "v_mul_f32 v129, v129, v132\n\t"
        "v_mul_f32 v130, v130, v132\n\t"
        "v_mul_f32 v135, v135, v132\n\t"
        "v_accvgpr_write_b32 a228, v128\n\t"
        "v_accvgpr_write_b32 a229, v129\n\t"
        "v_accvgpr_write_b32 a230, v130\n\t"
        "v_accvgpr_write_b32 a231, v135\n\t"
        "v_accvgpr_read_b32 v128, a232\n\t"
        "v_accvgpr_read_b32 v129, a233\n\t"
        "v_accvgpr_read_b32 v130, a234\n\t"
        "v_accvgpr_read_b32 v135, a235\n\t"
        "v_mul_f32 v128, v128, v133\n\t"
        "v_mul_f32 v129, v129, v133\n\t"
        "v_mul_f32 v130, v130, v133\n\t"
        "v_mul_f32 v135, v135, v133\n\t"
        "v_accvgpr_write_b32 a232, v128\n\t"
        "v_accvgpr_write_b32 a233, v129\n\t"
        "v_accvgpr_write_b32 a234, v130\n\t"
        "v_accvgpr_write_b32 a235, v135\n\t"
        "v_accvgpr_read_b32 v128, a236\n\t"
        "v_accvgpr_read_b32 v129, a237\n\t"
        "v_accvgpr_read_b32 v130, a238\n\t"
        "v_accvgpr_read_b32 v135, a239\n\t"
        "v_mul_f32 v128, v128, v134\n\t"
        "v_mul_f32 v129, v129, v134\n\t"
        "v_mul_f32 v130, v130, v134\n\t"
        "v_mul_f32 v135, v135, v134\n\t"
        "v_accvgpr_write_b32 a236, v128\n\t"
        "v_accvgpr_write_b32 a237, v129\n\t"
        "v_accvgpr_write_b32 a238, v130\n\t"
        "v_accvgpr_write_b32 a239, v135\n\t"
        "v_accvgpr_read_b32 v128, a240\n\t"
        "v_accvgpr_read_b32 v129, a241\n\t"
        "v_accvgpr_read_b32 v130, a242\n\t"
        "v_accvgpr_read_b32 v135, a243\n\t"
        "v_mul_f32 v128, v128, v131\n\t"
        "v_mul_f32 v129, v129, v131\n\t"
        "v_mul_f32 v130, v130, v131\n\t"
        "v_mul_f32 v135, v135, v131\n\t"
        "v_accvgpr_write_b32 a240, v128\n\t"
        "v_accvgpr_write_b32 a241, v129\n\t"
        "v_accvgpr_write_b32 a242, v130\n\t"
        "v_accvgpr_write_b32 a243, v135\n\t"
        "v_accvgpr_read_b32 v128, a244\n\t"
        "v_accvgpr_read_b32 v129, a245\n\t"
        "v_accvgpr_read_b32 v130, a246\n\t"
        "v_accvgpr_read_b32 v135, a247\n\t"
        "v_mul_f32 v128, v128, v132\n\t"
        "v_mul_f32 v129, v129, v132\n\t"
        "v_mul_f32 v130, v130, v132\n\t"
        "v_mul_f32 v135, v135, v132\n\t"
        "v_accvgpr_write_b32 a244, v128\n\t"
        "v_accvgpr_write_b32 a245, v129\n\t"
        "v_accvgpr_write_b32 a246, v130\n\t"
        "v_accvgpr_write_b32 a247, v135\n\t"
        "v_accvgpr_read_b32 v128, a248\n\t"
        "v_accvgpr_read_b32 v129, a249\n\t"
        "v_accvgpr_read_b32 v130, a250\n\t"
        "v_accvgpr_read_b32 v135, a251\n\t"
        "v_mul_f32 v128, v128, v133\n\t"
        "v_mul_f32 v129, v129, v133\n\t"
        "v_mul_f32 v130, v130, v133\n\t"
        "v_mul_f32 v135, v135, v133\n\t"
        "v_accvgpr_write_b32 a248, v128\n\t"
        "v_accvgpr_write_b32 a249, v129\n\t"
        "v_accvgpr_write_b32 a250, v130\n\t"
        "v_accvgpr_write_b32 a251, v135\n\t"
        "v_accvgpr_read_b32 v128, a252\n\t"
        "v_accvgpr_read_b32 v129, a253\n\t"
        "v_accvgpr_read_b32 v130, a254\n\t"
        "v_accvgpr_read_b32 v135, a255\n\t"
        "v_mul_f32 v128, v128, v134\n\t"
        "v_mul_f32 v129, v129, v134\n\t"
        "v_mul_f32 v130, v130, v134\n\t"
        "v_mul_f32 v135, v135, v134\n\t"
        "v_accvgpr_write_b32 a252, v128\n\t"
        "v_accvgpr_write_b32 a253, v129\n\t"
        "v_accvgpr_write_b32 a254, v130\n\t"
        "v_accvgpr_write_b32 a255, v135\n\t"
        "s_nop 3\n\t"
        "s_cmp_eq_u32 s66, 1\n\t"
        "s_cbranch_scc1 RRET1_%=\n\t"
        "s_branch RRET3_%=\n\t"
        "RSTUB1_%=:\n\t"
        "s_mov_b32 s66, 1\n\t"
        "s_branch RARE1_%=\n\t"
        "RSTUB3_%=:\n\t"
        "s_mov_b32 s66, 3\n\t"
        "s_branch RARE1_%=\n\t"
        "END_%=:\n\t"
        : [flag] "=&s"(flag)
        : [q] "s"(q), [k] "s"(kc), [v] "s"(vc), [bt] "s"(bt), [cl] "s"(cl), [o] "s"(out), [ws] "s"(ws), [kvb] "s"(kvbytes), [blk] "s"(blk), [wid] "s"(wid), [lds] "s"(ldsb)
        : "v0", "v1", "v2", "v3", "v4", "v5", "v6", "v7", "v8", "v9", "v10", "v11", "v12", "v13", "v14", "v15", "v16", "v17", "v18", "v19", "v20", "v21", "v22", "v23", "v24", "v25", "v26", "v27", "v28", "v29", "v30", "v31", "v32", "v33", "v34", "v35", "v36", "v37", "v38", "v39", "v40", "v41", "v42", "v43", "v44", "v45", "v46", "v47", "v48", "v49", "v50", "v51", "v52", "v53", "v54", "v55", "v56", "v57", "v58", "v59", "v60", "v61", "v62", "v63", "v64", "v65", "v66", "v67", "v68", "v69", "v70", "v71", "v72", "v73", "v74", "v75", "v76", "v77", "v78", "v79", "v80", "v81", "v82", "v83", "v84", "v85", "v86", "v87", "v88", "v89", "v90", "v91", "v92", "v93", "v94", "v95", "v96", "v97", "v98", "v99", "v100", "v101", "v102", "v103", "v104", "v105", "v106", "v107", "v108", "v109", "v110", "v111", "v112", "v113", "v114", "v115", "v116", "v117", "v118", "v119", "v120", "v121", "v122", "v123", "v124", "v125", "v126", "v127", "v128", "v129", "v130", "v131", "v132", "v133", "v134", "v135", "v136", "v137", "v138", "v139", "v140", "v141", "v142", "v143", "v144", "v145", "v146", "v147", "v148", "v149", "v150", "v151", "v152", "v153", "v154", "v155", "v156", "v157", "v158", "v159", "v160", "v161", "v162", "v163", "v164", "v165", "v166", "v167", "v168", "v169", "v170", "v171", "v172", "v173", "v174", "v175", "v176", "v177", "v178", "v179", "v180", "v181", "v182", "v183", "v184", "v185", "v186", "v187", "v188", "v189", "v190", "v191", "v192", "v193", "v194", "v195", "v196", "v197", "v198", "v199", "v200", "v201", "v202", "v203", "v204", "v205", "v206", "v207", "v208", "v209", "v210", "v211", "v212", "v213", "v214", "v215", "v216", "v217", "v218", "v219", "v220", "v221", "v222", "v223", "v224", "v225", "v226", "v227", "v228", "v229", "v230", "v231", "v232", "v233", "v234", "v235", "v236", "v237", "v238", "v239", "v240", "v241", "v242", "v243", "v244", "v245", "v246", "v247", "v248", "v249", "v250", "v251", "v252", "v253", "v254", "v255", "a0", "a1", "a2", "a3", "a4", "a5", "a6", "a7", "a8", "a9", "a10", "a11", "a12", "a13", "a14", "a15", "a16", "a17", "a18", "a19", "a20", "a21", "a22", "a23", "a24", "a25", "a26", "a27", "a28", "a29", "a30", "a31", "a32", "a33", "a34", "a35", "a36", "a37", "a38", "a39", "a40", "a41", "a42", "a43", "a44", "a45", "a46", "a47", "a48", "a49", "a50", "a51", "a52", "a53", "a54", "a55", "a56", "a57", "a58", "a59", "a60", "a61", "a62", "a63", "a64", "a65", "a66", "a67", "a68", "a69", "a70", "a71", "a72", "a73", "a74", "a75", "a76", "a77", "a78", "a79", "a80", "a81", "a82", "a83", "a84", "a85", "a86", "a87", "a88", "a89", "a90", "a91", "a92", "a93", "a94", "a95", "a96", "a97", "a98", "a99", "a100", "a101", "a102", "a103", "a104", "a105", "a106", "a107", "a108", "a109", "a110", "a111", "a112", "a113", "a114", "a115", "a116", "a117", "a118", "a119", "a120", "a121", "a122", "a123", "a124", "a125", "a126", "a127", "a128", "a129", "a130", "a131", "a132", "a133", "a134", "a135", "a136", "a137", "a138", "a139", "a140", "a141", "a142", "a143", "a144", "a145", "a146", "a147", "a148", "a149", "a150", "a151", "a152", "a153", "a154", "a155", "a156", "a157", "a158", "a159", "a160", "a161", "a162", "a163", "a164", "a165", "a166", "a167", "a168", "a169", "a170", "a171", "a172", "a173", "a174", "a175", "a176", "a177", "a178", "a179", "a180", "a181", "a182", "a183", "a184", "a185", "a186", "a187", "a188", "a189", "a190", "a191", "a192", "a193", "a194", "a195", "a196", "a197", "a198", "a199", "a200", "a201", "a202", "a203", "a204", "a205", "a206", "a207", "a208", "a209", "a210", "a211", "a212", "a213", "a214", "a215", "a216", "a217", "a218", "a219", "a220", "a221", "a222", "a223", "a224", "a225", "a226", "a227", "a228", "a229", "a230", "a231", "a232", "a233", "a234", "a235", "a236", "a237", "a238", "a239", "a240", "a241", "a242", "a243", "a244", "a245", "a246", "a247", "a248", "a249", "a250", "a251", "a252", "a253", "a254", "a255", "s40", "s41", "s42", "s43", "s44", "s45", "s46", "s47", "s48", "s49", "s50", "s51", "s52", "s53", "s54", "s55", "s56", "s57", "s58", "s59", "s60", "s61", "s62", "s63", "s64", "s65", "s66", "s67", "s68", "s69", "s70", "s71", "s72", "s73", "s74", "s75", "s76", "s77", "s78", "s79", "s80", "s81", "s82", "s83", "s84", "s85", "s86", "s87", "s88", "s89", "s90", "s91", "s92", "s93", "s94", "s95", "s96", "s97", "s98", "s99", "vcc", "scc", "m0", "memory");
    if (flag) {
        const int kvh = blk & 7, idx = blk >> 3, h = 2 * kvh + (idx >> 4), qblk = idx & 15;
        int lane;
        asm volatile("v_mbcnt_lo_u32_b32 %0, -1, 0\n\tv_mbcnt_hi_u32_b32 %0, -1, %0" : "=v"(lane));
        slow_rows(q, kc, vc, bt, cl[0], out, h, 128 * qblk + 64 * (wid >> 1), 64, 128 * (wid & 1), lane);
    }
}

extern "C" void kernel_launch(void* const* d_in, const int* in_sizes, int n_in, void* d_out, int out_size, void* d_ws, size_t ws_size,
                              hipStream_t stream) {
    (void)n_in; (void)out_size; (void)ws_size;
    const unsigned kvbytes = (unsigned)in_sizes[1] * 2u;
    paged_attn_prompt_kernel<<<256, 256, 0, stream>>>((const unsigned short*)d_in[0], (const unsigned short*)d_in[1], (const unsigned short*)d_in[2],
                                                      (const int*)d_in[3], (const int*)d_in[4], (unsigned short*)d_out, d_ws, kvbytes);
}
```
